```python
import torch

def _softmax(x, axis=-1):
    return torch.softmax(x, dim=axis)
def _gelu(x):
    return torch.nn.functional.gelu(x, approximate="tanh")
def _silu(x):
    return torch.nn.functional.silu(x)
def _sigmoid(x):
    return torch.sigmoid(x)
def _layernorm(x, axis=-1, eps=1e-5):
    m = x.mean(dim=axis, keepdim=True)
    v = x.var(dim=axis, keepdim=True, unbiased=False)
    return (x - m) / torch.sqrt(v + eps)
def _scatter_add(tgt, idx, src, axis=0):
    return tgt.clone().index_add_(axis, idx, src)
def _segment_sum(src, seg, n):
    out = torch.zeros((n,) + src.shape[1:], dtype=src.dtype)
    return out.index_add_(0, seg, src)
def _pad(x, pw):
    flat = tuple(v for pair in reversed(pw) for v in pair)
    return torch.nn.functional.pad(x, flat)

kernel_name = 'parallel_sum__softmax_full_mha__glu_gated_swiglu__prompt'

def setup_inputs():

    _w = (lambda a: (lambda t: t.to(torch.bfloat16) if t.is_floating_point() else t)(torch.as_tensor(a)))
    def _A_setup():
        B, S, D = 1, 2048, 640
        return {
            "x": _w((lambda *s: torch.randn(*s, dtype=torch.bfloat16) * 0.05)(B, S, D)),
            "wqkv": _w((lambda *s: torch.randn(*s, dtype=torch.bfloat16) * 0.05)(D, 3 * D)),
            "wo": _w((lambda *s: torch.randn(*s, dtype=torch.bfloat16) * 0.05)(D, D)),
        }
    _A_in = _A_setup()

    def _B_setup():
        B, S, D, F = 1, 2048, 640, 1536
        return {
            "x": _w((lambda *s: torch.randn(*s, dtype=torch.bfloat16) * 0.05)(B, S, D)),
            "w1": _w((lambda *s: torch.randn(*s, dtype=torch.bfloat16) * 0.4)(D, F) * (2.0 / D) ** 0.5),
            "w3": _w((lambda *s: torch.randn(*s, dtype=torch.bfloat16) * 0.4)(D, F) * (2.0 / D) ** 0.5),
            "w2": _w((lambda *s: torch.randn(*s, dtype=torch.bfloat16) * 0.4)(F, D) * (2.0 / F) ** 0.5),
        }
    _B_in = _B_setup()

    return {
        "A_x": _A_in["x"],
        "A_wqkv": _A_in["wqkv"],
        "A_wo": _A_in["wo"],
        "B_x": _B_in["x"],
        "B_w1": _B_in["w1"],
        "B_w3": _B_in["w3"],
        "B_w2": _B_in["w2"],
    }

def reference(A_x, A_wqkv, A_wo, B_x, B_w1, B_w3, B_w2):
    def _A_ref(x, wqkv, wo):
        B, S, D = x.shape
        H = 32; Dh = D // H
        qkv = (lambda x, *d: x.permute(*d))((x @ wqkv).reshape(B, S, 3, H, Dh), 2, 0, 3, 1, 4)
        q, k, v = qkv[0], qkv[1], qkv[2]
        scale = Dh ** -0.5
        scores = torch.einsum("bhqd,bhkd->bhqk", q, k) * scale
        p = _softmax(scores, axis=-1)
        o = torch.einsum("bhqk,bhkd->bhqd", p, v)
        return (lambda x, *d: x.permute(*d))(o, 0, 2, 1, 3).reshape(B, S, D) @ wo

    def _B_ref(x, w1, w3, w2):
        gate = torch.einsum("bsd,df->bsf", x, w1)
        up = torch.einsum("bsd,df->bsf", x, w3)
        h = _silu(gate) * up
        out = torch.einsum("bsf,fd->bsd", h, w2)
        return out

    x = A_x
    xn = _layernorm(x, axis=-1)
    return x + _A_ref(xn, A_wqkv, A_wo) + _B_ref(xn, B_w1, B_w3, B_w2)
```

```cpp
#include <hip/hip_runtime.h>
#include <stdint.h>

typedef __attribute__((ext_vector_type(8))) short bf16x8;
typedef __attribute__((ext_vector_type(4))) short s16x4;
typedef __attribute__((ext_vector_type(2))) __bf16 bf16x2;
typedef __attribute__((ext_vector_type(16))) float f32x16;
typedef __attribute__((ext_vector_type(8))) float f32x8;
typedef __attribute__((ext_vector_type(4))) float f32x4;
typedef __attribute__((ext_vector_type(2))) float f32x2;
typedef __attribute__((ext_vector_type(4))) unsigned u32x4;
typedef __attribute__((ext_vector_type(2))) unsigned u32x2;
typedef unsigned short u16;
#define LAS __attribute__((address_space(3)))
#define GAS __attribute__((address_space(1)))
#define DI __device__ __forceinline__
#define MFMA32(a, b, c) __builtin_amdgcn_mfma_f32_32x32x16_bf16((a), (b), (c), 0, 0, 0)
#define WAIT_V(n) asm volatile("s_waitcnt vmcnt(" #n ")" ::: "memory")
#define WAIT_L0() asm volatile("s_waitcnt lgkmcnt(0)" ::: "memory")
#define BAR() asm volatile("s_barrier" ::: "memory")

constexpr int S_ = 2048, D_ = 640, NH = 32, F_ = 1536, NQKV = 1920;
constexpr unsigned KV_CHUNK = 21504, KV_HEAD = 8 * KV_CHUNK;
constexpr unsigned WS_QI = 4096;
constexpr unsigned WS_KV = WS_QI + NH * 64 * 1280;
constexpr unsigned WS_XN = WS_KV + NH * KV_HEAD;
constexpr unsigned WS_X2 = WS_XN + 80 * S_ * 16;
constexpr unsigned WS_WT = WS_X2 + 272 * S_ * 16;
constexpr unsigned WS_END = WS_WT + 8 * 68 * 5120;
#define QS 128
#define HS 8
constexpr unsigned LS = 8, OS = 128;
constexpr unsigned WS_EP = WS_END, WS_LNF = WS_EP + 256, WS_QKF = WS_LNF + 256 * 128, WS_HF = WS_QKF + 256 * 128, WS_OF = WS_HF + 256 * 128, WS_GF = WS_OF + 256 * 128,
                   WS_STAT = WS_GF + 256 * 128;
constexpr unsigned WS_ALL = WS_STAT + S_ * 16;
constexpr int SC1 = 16;
constexpr float QSCALE = 0.22360679774997896f * 1.4426950408889634f;

#define STAMP(ws, kid, i) do {} while (0)
DI unsigned pk_bf16(float lo, float hi) { bf16x2 v; v[0] = (__bf16)lo; v[1] = (__bf16)hi; return __builtin_bit_cast(unsigned, v); }
DI float bf_round(float x) { return __builtin_bit_cast(float, pk_bf16(0.f, x) & 0xffff0000u); }
DI float bf_lo(unsigned u) { return __builtin_bit_cast(float, u << 16); }
DI float bf_hi(unsigned u) { return __builtin_bit_cast(float, u & 0xffff0000u); }
DI float dot2(unsigned a, unsigned b, float c) { return __builtin_amdgcn_fdot2_f32_bf16(__builtin_bit_cast(bf16x2, a), __builtin_bit_cast(bf16x2, b), c, false); }
DI float swap32_add(float x) { auto r = __builtin_amdgcn_permlane32_swap(__float_as_uint(x), __float_as_uint(x), false, false); return __uint_as_float(r[0]) + __uint_as_float(r[1]); }
DI float swap32_max(float x) { auto r = __builtin_amdgcn_permlane32_swap(__float_as_uint(x), __float_as_uint(x), false, false); return __builtin_fmaxf(__uint_as_float(r[0]), __uint_as_float(r[1])); }
DI void glds16c(const void* base, unsigned voff, unsigned lds) {
  asm volatile("s_mov_b32 m0, %2\n\ts_nop 0\n\tglobal_load_lds_dwordx4 %0, %1 sc1" :: "v"(voff), "s"(base), "s"(lds) : "memory");
}
DI void glds16(const void* base, unsigned voff, unsigned lds) {
  asm volatile("s_mov_b32 m0, %2\n\ts_nop 0\n\tglobal_load_lds_dwordx4 %0, %1" :: "v"(voff), "s"(base), "s"(lds) : "memory");
}
DI unsigned lds_addr(const LAS char* p) { return (unsigned)(uintptr_t)p; }
DI bf16x8 lds_b128(const LAS char* p) { return *(const LAS bf16x8*)p; }
DI bf16x8 lds_tr2(const LAS char* p0, const LAS char* p1) {
  s16x4 a = __builtin_amdgcn_ds_read_tr16_b64_v4i16((LAS s16x4*)p0), b = __builtin_amdgcn_ds_read_tr16_b64_v4i16((LAS s16x4*)p1);
  return __builtin_shufflevector(a, b, 0, 1, 2, 3, 4, 5, 6, 7);
}
#define PRIO_GU 0
#define PRIO_DN 0
#define PRIO_LN 2
#define ROT 1
#define WARM 0
#define GU_LA 2
#define POLL_SLEEP 6
#define SPF 0
#define ROTG 0
#define PRIO_A 1
#define GATE2 1000
#define PRIO_TL 3
#define GPOS 1
#define CODE_KB 0
#define QDELAY 0
#define HFW 0
#define TLN 700
#define TGF 0
#define ROTU 0
#define LNMAP 1
#define GATE1 0
#define GATE 0
typedef unsigned long long u64;
typedef __amdgpu_buffer_rsrc_t rsrc_t;
DI rsrc_t ws_rsrc(char* ws) { return __builtin_amdgcn_make_buffer_rsrc(ws, 0, WS_ALL, 0x00020000); }
DI void st128(rsrc_t rs, unsigned voff, unsigned soff, u32x4 v) {
  __builtin_amdgcn_raw_buffer_store_b128(v, rs, voff, soff, SC1);
  asm volatile("s_nop 1" :: "v"(v) : "memory");
}
DI void st64(rsrc_t rs, unsigned voff, unsigned soff, u32x2 v) { __builtin_amdgcn_raw_buffer_store_b64(v, rs, voff, soff, SC1); }
DI u32x4 ld128(rsrc_t rs, unsigned voff, unsigned soff) { return __builtin_amdgcn_raw_buffer_load_b128(rs, voff, soff, SC1); }
DI u32x2 ld64(rsrc_t rs, unsigned voff, unsigned soff) { return __builtin_amdgcn_raw_buffer_load_b64(rs, voff, soff, SC1); }
DI u64 ld_flag(const char* ws, unsigned off) { return __hip_atomic_load((const GAS u64*)(ws + off), __ATOMIC_RELAXED, __HIP_MEMORY_SCOPE_AGENT); }
DI void st_flag(char* ws, unsigned off, u64 v) { __hip_atomic_store((GAS u64*)(ws + off), v, __ATOMIC_RELAXED, __HIP_MEMORY_SCOPE_AGENT); }
DI int fresh_lane() { int l; asm volatile("v_mbcnt_lo_u32_b32 %0, -1, 0\n\tv_mbcnt_hi_u32_b32 %0, -1, %0" : "=v"(l)); return l; }
constexpr int SPIN_MAX = 1 << 16;
DI u64 uniform64(u64 v) { return ((u64)(unsigned)__builtin_amdgcn_readfirstlane((unsigned)(v >> 32)) << 32) | (unsigned)__builtin_amdgcn_readfirstlane((unsigned)v); }
DI void publish(char* ws, unsigned off, u64 tag, int w) { WAIT_V(0); BAR(); if (w == 0 && fresh_lane() == 0) st_flag(ws, off, tag); }
DI void await(const char* ws, unsigned off0, unsigned off1, u64 tag, int w) {
  if (w == 0)
#pragma unroll 1
    for (int it = 0; it < SPIN_MAX; ++it) {
      const u64 a = ld_flag(ws, off0), b = off1 == ~0u ? a : ld_flag(ws, off1);
      if (__all(a == tag && b == tag)) break;
      __builtin_amdgcn_s_sleep(POLL_SLEEP);
    }
  WAIT_V(0); BAR();
}
DI f32x2 ln_scalars(float mean, float var) {
  const float m = bf_round(mean), v = bf_round(var), t = bf_round(v + 1e-5f), s = bf_round(__builtin_amdgcn_sqrtf(t));
  return f32x2{s, m};
}

DI float red8(float v) {
  v += __builtin_bit_cast(float, __builtin_amdgcn_update_dpp(0, __builtin_bit_cast(int, v), 0x128  , 0xf, 0xf, true));
  auto a = __builtin_amdgcn_permlane16_swap(__float_as_uint(v), __float_as_uint(v), false, false);
  return swap32_add(__uint_as_float(a[0]) + __uint_as_float(a[1]));
}
DI void ln_rows(const u16* x, char* ws, int tok0, LAS f32x2* red, int w, u64& tag) {
  const int t = threadIdx.x, tok = t & 7, u = t >> 3;
  u32x2 v[5];
#pragma unroll
  for (int i = 0; i < 5; ++i) v[i] = *(const u32x2*)(x + (tok0 + tok) * D_ + u * 4 + 128 * i);
  const rsrc_t rs = ws_rsrc(ws);
  tag = uniform64(ld_flag(ws, WS_EP)) + 1;
  f32x2 f[10], s = {0.f, 0.f}, q = {0.f, 0.f};
#pragma unroll
  for (int i = 0; i < 10; ++i) { f[i] = f32x2{bf_lo(v[i >> 1][i & 1]), bf_hi(v[i >> 1][i & 1])}; s += f[i]; q += f[i] * f[i]; }
  const f32x2 part = {red8(s[0] + s[1]), red8(q[0] + q[1])};
  if ((t & 63) < 8) red[w * 8 + tok] = part;
  WAIT_L0(); BAR();
  const f32x2 tot = (red[tok] + red[8 + tok]) + (red[16 + tok] + red[24 + tok]);
  const float mean = tot[0] * (1.0f / D_);
  const f32x2 sc = ln_scalars(mean, __builtin_fmaxf(tot[1] * (1.0f / D_) - mean * mean, 0.f));
  const unsigned sm2 = pk_bf16(sc[0], sc[1]);
  st128(rs, t < 8 ? tok * 16 : WS_ALL, WS_STAT + tok0 * 16, u32x4{(unsigned)tag, sm2, (unsigned)(tag >> 32), sm2});
  const float ri = __builtin_amdgcn_rcpf(sc[0]);
#pragma unroll
  for (int i = 0; i < 5; ++i) {
    const f32x2 a0 = f[2 * i] - sc[1], b0 = f[2 * i + 1] - sc[1];
    unsigned ua = pk_bf16(a0[0], a0[1]), ub = pk_bf16(b0[0], b0[1]);
    asm("" : "+v"(ua), "+v"(ub));
    const f32x2 a = f32x2{bf_lo(ua), bf_hi(ua)} * ri, b = f32x2{bf_lo(ub), bf_hi(ub)} * ri;
    st64(rs, ((u >> 1) * S_ + tok) * 16 + (u & 1) * 8, WS_XN + (16 * i * S_ + tok0) * 16, u32x2{pk_bf16(a[0], a[1]), pk_bf16(b[0], b[1])});
  }
}

constexpr int QKV_W = 49152;
DI unsigned kv_tile_off(int tile) { return ((tile & 31) >> 2) * KV_CHUNK + (tile >> 5) * 10752 + (tile & 3) * 2624; }
struct QkvSrc { const char* xb; const char* wb; unsigned xe, xo, wv, ldx, ldw, k1; };
DI QkvSrc qkv_start(const u16* x, const u16* wqkv, LAS char* sm, int tok0, int col0, int k0, int w, int lane) {
  QkvSrc a;
  a.k1 = k0 == 9 ? 0 : k0 + 1;
  a.ldx = lds_addr(sm) + w * 4096; a.ldw = lds_addr(sm) + QKV_W + w * 2048;
  a.xb = (const char*)(x + (tok0 + w * 32) * D_);
  a.wb = (const char*)(wqkv + w * 8 * NQKV + col0);
  a.xe = (lane >> 3) * (D_ * 2) + (((lane & 7) ^ (lane >> 4)) * 16); a.xo = a.xe ^ 64;
  a.wv = (lane >> 4) * (NQKV * 2) + (((lane & 15) ^ (4 * (lane >> 4))) * 16);
  const char* px = a.xb + k0 * 128; const char* pw = a.wb + k0 * 64 * NQKV * 2;
  glds16(px, a.xe, a.ldx); glds16(px + 10240, a.xo, a.ldx + 1024);
  glds16(pw, a.wv, a.ldw); glds16(pw + 4 * NQKV * 2, a.wv, a.ldw + 1024);
  glds16(px + 20480, a.xe, a.ldx + 2048); glds16(px + 30720, a.xo, a.ldx + 3072);
  glds16(pw + 32 * NQKV * 2, a.wv, a.ldw + 8192); glds16(pw + 36 * NQKV * 2, a.wv, a.ldw + 8192 + 1024);
  return a;
}
template <bool VT>
DI u64 qkv_tile(const QkvSrc& a, LAS char* sm, char* ws, int tok0, int col0, int w, int lane) {
  const int wr = w >> 1, wc = w & 1, r = lane & 31, h = lane >> 5;
  const unsigned l0 = lds_addr(sm);
  unsigned xa[4], wa[2];
#pragma unroll
  for (int s = 0; s < 4; ++s) xa[s] = l0 + (64 * wr + 32 * wc + r) * 128 + (((2 * s + h) ^ ((r >> 1) & 7)) * 16);
  const int q = (lane & 15) >> 2, pp = lane & 3, blk = (lane >> 4) & 1;
#pragma unroll
  for (int j = 0; j < 2; ++j) wa[j] = l0 + QKV_W + (8 * h + q) * 256 + ((4 * ((2 * wc + (j ^ wr)) ^ q) + 2 * blk + (pp >> 1)) * 16) + 8 * (pp & 1);
  const unsigned sv = (blk == (lane & 1)) ? 0x3f803f80u : 0u;
  const u32x4 sel = {sv, sv, sv, sv};
  const bool isq = col0 < 640;
  unsigned so[2][4];
#pragma unroll
  for (int j = 0; j < 2; ++j) {
    const int ft = 2 * wc + (j ^ wr);
    if (VT) { const unsigned f = col0 - 1280 + 32 * ft + r, head = f / 20u, d = f - head * 20u; so[j][0] = head * KV_HEAD + 1280 + h * 352 + d * 16; asm volatile("" : "+v"(so[j][0])); }
    else {
#pragma unroll
      for (int g = 0; g < 4; ++g) {
        const unsigned gi = ((isq ? col0 : col0 - 640) >> 2) + 8 * ft + h + 2 * g, head = gi / 5u, G = gi - head * 5u;
        so[j][g] = head * (isq ? 64 * 1280 : KV_HEAD) + G * 256 + r * 8;
        asm volatile("" : "+v"(so[j][g]));
      }
    }
  }
  unsigned base[2];
#pragma unroll
  for (int i = 0; i < 2; ++i) {
    const int tile = (tok0 >> 5) + 2 * wr + (i ^ wc);
    base[i] = __builtin_amdgcn_readfirstlane(isq ? WS_QI + tile * 1280 : WS_KV + kv_tile_off(tile));
    asm volatile("" : "+s"(base[i]));
  }
  unsigned la[2] = {(unsigned)(VT ? 32 * (2 * wr + wc) + 4 * h : 32 * (2 * wr + wc) + r) * 8, (unsigned)(VT ? 32 * (2 * wc + wr) + r : 32 * (2 * wc + wr) + 4 * h) * 4};
  asm volatile("" : "+v"(la[0]), "+v"(la[1]));
  const rsrc_t rs = ws_rsrc(ws);
  if (VT) {
    const int t = threadIdx.x, f0 = col0 - 1280, hd = (f0 + 19) / 20 + (t >> 4), s = (tok0 >> 4) + ((t >> 1) & 7);
    if (t < 112 && hd * 20 < f0 + 128) {
      const unsigned v = (t & 1) ? 0u : 0x3f803f80u;
      st128(rs, hd * KV_HEAD + kv_tile_off(s >> 1) + 1280 + (s & 1) * 672 + 320 + (t & 1) * 16, WS_KV, u32x4{v, v, v, v});
    }
  }
  f32x16 acc[2][2];
  unsigned tag0, tag1;

  STAMP(ws, 0, 1);
#define DBG_OP
#define QKV_OPS : "=&{v[64:79]}"(acc[0][0]), "=&{v[80:95]}"(acc[0][1]), "=&{v[96:111]}"(acc[1][0]), "=&{v[112:127]}"(acc[1][1]), [tag0] "=&s"(tag0), [tag1] "=&s"(tag1) DBG_OP \
      : [xa0] "v"(xa[0]), [xa1] "v"(xa[1]), [xa2] "v"(xa[2]), [xa3] "v"(xa[3]), [wa0] "v"(wa[0]), [wa1] "v"(wa[1]), [xe] "v"(a.xe), [xo] "v"(a.xo), [wv] "v"(a.wv), \
        [sel] "v"(sel), [gv] "v"((threadIdx.x & 127) * 16), [gb] "s"(ws + WS_STAT + tok0 * 16), \
        [sw] "v"(l0 + 16384 + (threadIdx.x & 127) * 8), [cw] "v"(l0 + 16384 + 1024 + (32 * (2 * wc + wr) + r) * 4), [qs] "s"(isq ? QSCALE : 1.0f), [ep] "s"(ws + WS_EP), \
        [xb0] "s"((unsigned)(uintptr_t)a.xb), [xb1] "s"((unsigned)((uintptr_t)a.xb >> 32)), [wb0] "s"((unsigned)(uintptr_t)a.wb), [wb1] "s"((unsigned)((uintptr_t)a.wb >> 32)), \
        [k1] "s"(a.k1), [ldx] "s"(a.ldx), [ldxe] "s"(a.ldx + 49152), [ldw] "s"(a.ldw)
  if (VT) {
    asm volatile(
      "s_mul_i32 s40, %[k1], 245760\n"
      "s_lshl_b32 s42, %[k1], 7\n"
      "s_mov_b32 s44, 0\n"
      "s_add_u32 s45, %[ldx], 16384\n"
      "s_mov_b32 s36, 3\n"
      "v_add_u32 v168, s44, %[xa0]\n"
      "v_add_u32 v169, s44, %[xa1]\n"
      "v_add_u32 v170, s44, %[xa2]\n"
      "v_add_u32 v171, s44, %[xa3]\n"
      "v_xor_b32 v172, 0x1000, v168\n"
      "v_xor_b32 v173, 0x1000, v169\n"
      "v_xor_b32 v174, 0x1000, v170\n"
      "v_xor_b32 v175, 0x1000, v171\n"
      "v_mov_b32 v64, 0\n"
      "v_mov_b32 v65, 0\n"
      "v_mov_b32 v66, 0\n"
      "v_mov_b32 v67, 0\n"
      "v_mov_b32 v68, 0\n"
      "v_mov_b32 v69, 0\n"
      "v_mov_b32 v70, 0\n"
      "v_mov_b32 v71, 0\n"
      "v_mov_b32 v72, 0\n"
      "v_mov_b32 v73, 0\n"
      "v_mov_b32 v74, 0\n"
      "v_mov_b32 v75, 0\n"
      "v_mov_b32 v76, 0\n"
      "v_mov_b32 v77, 0\n"
      "v_mov_b32 v78, 0\n"
      "v_mov_b32 v79, 0\n"
      "v_mov_b32 v80, 0\n"
      "v_mov_b32 v81, 0\n"
      "v_mov_b32 v82, 0\n"
      "v_mov_b32 v83, 0\n"
      "v_mov_b32 v84, 0\n"
      "v_mov_b32 v85, 0\n"
      "v_mov_b32 v86, 0\n"
      "v_mov_b32 v87, 0\n"
      "v_mov_b32 v88, 0\n"
      "v_mov_b32 v89, 0\n"
      "v_mov_b32 v90, 0\n"
      "v_mov_b32 v91, 0\n"
      "v_mov_b32 v92, 0\n"
      "v_mov_b32 v93, 0\n"
      "v_mov_b32 v94, 0\n"
      "v_mov_b32 v95, 0\n"
      "v_mov_b32 v96, 0\n"
      "v_mov_b32 v97, 0\n"
      "v_mov_b32 v98, 0\n"
      "v_mov_b32 v99, 0\n"
      "v_mov_b32 v100, 0\n"
      "v_mov_b32 v101, 0\n"
      "v_mov_b32 v102, 0\n"
      "v_mov_b32 v103, 0\n"
      "v_mov_b32 v104, 0\n"
      "v_mov_b32 v105, 0\n"
      "v_mov_b32 v106, 0\n"
      "v_mov_b32 v107, 0\n"
      "v_mov_b32 v108, 0\n"
      "v_mov_b32 v109, 0\n"
      "v_mov_b32 v110, 0\n"
      "v_mov_b32 v111, 0\n"
      "v_mov_b32 v112, 0\n"
      "v_mov_b32 v113, 0\n"
      "v_mov_b32 v114, 0\n"
      "v_mov_b32 v115, 0\n"
      "v_mov_b32 v116, 0\n"
      "v_mov_b32 v117, 0\n"
      "v_mov_b32 v118, 0\n"
      "v_mov_b32 v119, 0\n"
      "v_mov_b32 v120, 0\n"
      "v_mov_b32 v121, 0\n"
      "v_mov_b32 v122, 0\n"
      "v_mov_b32 v123, 0\n"
      "v_mov_b32 v124, 0\n"
      "v_mov_b32 v125, 0\n"
      "v_mov_b32 v126, 0\n"
      "v_mov_b32 v127, 0\n"
      "v_mov_b32 v128, 0\n"
      "v_mov_b32 v129, 0\n"
      "v_mov_b32 v130, 0\n"
      "v_mov_b32 v131, 0\n"
      "s_waitcnt vmcnt(2)\n"
      "s_barrier\n"
      "ds_read_b128 v[136:139], v168\n"
      "ds_read_b64_tr_b16 v[144:145], %[wa0] offset:0\n"
      "ds_read_b64_tr_b16 v[146:147], %[wa0] offset:1024\n"
      "ds_read_b64_tr_b16 v[148:149], %[wa1] offset:0\n"
      "ds_read_b64_tr_b16 v[150:151], %[wa1] offset:1024\n"
      "ds_read_b128 v[140:143], v172\n"
      "s_waitcnt lgkmcnt(3)\n"
      "v_mfma_f32_32x32x16_bf16 v[64:79], v[136:139], v[144:147], v[64:79]\n"
      "ds_read_b128 v[152:155], v169\n"
      "ds_read_b64_tr_b16 v[160:161], %[wa0] offset:4096\n"
      "ds_read_b64_tr_b16 v[162:163], %[wa0] offset:5120\n"
      "s_waitcnt lgkmcnt(4)\n"
      "v_mfma_f32_32x32x16_bf16 v[80:95], v[136:139], v[148:151], v[80:95]\n"
      "s_add_u32 m0, s45, 0\n"
      "s_add_u32 s43, s42, 0\n"
      "s_add_u32 s46, %[xb0], s43\n"
      "s_addc_u32 s47, %[xb1], 0\n"
      "global_load_lds_dwordx4 %[xe], s[46:47]\n"
      "s_add_u32 m0, m0, 0x400\n"
      "s_add_u32 s46, s46, 10240\n"
      "s_addc_u32 s47, s47, 0\n"
      "global_load_lds_dwordx4 %[xo], s[46:47]\n"
      "s_waitcnt lgkmcnt(3)\n"
      "v_mfma_f32_32x32x16_bf16 v[112:127], v[140:143], v[148:151], v[112:127]\n"
      "ds_read_b64_tr_b16 v[164:165], %[wa1] offset:4096\n"
      "ds_read_b64_tr_b16 v[166:167], %[wa1] offset:5120\n"
      "ds_read_b128 v[156:159], v173\n"
      "v_mfma_f32_32x32x16_bf16 v[96:111], v[140:143], v[144:147], v[96:111]\n"
      "s_add_u32 m0, s45, 2048\n"
      "s_add_u32 s43, s42, 20480\n"
      "s_add_u32 s46, %[xb0], s43\n"
      "s_addc_u32 s47, %[xb1], 0\n"
      "global_load_lds_dwordx4 %[xe], s[46:47]\n"
      "s_add_u32 m0, m0, 0x400\n"
      "s_add_u32 s46, s46, 10240\n"
      "s_addc_u32 s47, s47, 0\n"
      "global_load_lds_dwordx4 %[xo], s[46:47]\n"
      "s_add_u32 s42, s42, 128\n"
      "s_cmp_eq_u32 s42, 1280\n"
      "s_cselect_b32 s42, 0, s42\n"
      "s_add_u32 s45, s45, 16384\n"
      "s_cmp_eq_u32 s45, %[ldxe]\n"
      "s_cselect_b32 s45, %[ldx], s45\n"
      "v_mfma_f32_16x16x32_bf16 v[128:131], %[sel], v[144:147], v[128:131]\n"
      "s_waitcnt vmcnt(4) lgkmcnt(0)\n"
      "s_barrier\n"
      "v_mfma_f32_16x16x32_bf16 v[128:131], %[sel], v[160:163], v[128:131]\n"
      "v_mfma_f32_32x32x16_bf16 v[64:79], v[152:155], v[160:163], v[64:79]\n"
      "ds_read_b128 v[136:139], v170\n"
      "ds_read_b64_tr_b16 v[144:145], %[wa0] offset:8192\n"
      "ds_read_b64_tr_b16 v[146:147], %[wa0] offset:9216\n"
      "v_mfma_f32_32x32x16_bf16 v[80:95], v[152:155], v[164:167], v[80:95]\n"
      "s_add_u32 m0, %[ldw], 16384\n"
      "s_add_u32 s46, %[wb0], s40\n"
      "s_addc_u32 s47, %[wb1], 0\n"
      "global_load_lds_dwordx4 %[wv], s[46:47]\n"
      "s_add_u32 m0, m0, 0x400\n"
      "s_add_u32 s46, s46, 15360\n"
      "s_addc_u32 s47, s47, 0\n"
      "global_load_lds_dwordx4 %[wv], s[46:47]\n"
      "s_add_u32 s40, s40, 122880\n"
      "s_cmp_eq_u32 s40, 2457600\n"
      "s_cselect_b32 s40, 0, s40\n"
      "v_mfma_f32_32x32x16_bf16 v[112:127], v[156:159], v[164:167], v[112:127]\n"
      "ds_read_b64_tr_b16 v[148:149], %[wa1] offset:8192\n"
      "ds_read_b64_tr_b16 v[150:151], %[wa1] offset:9216\n"
      "ds_read_b128 v[140:143], v174\n"
      "v_mfma_f32_32x32x16_bf16 v[96:111], v[156:159], v[160:163], v[96:111]\n"
      "s_add_u32 m0, %[ldw], 24576\n"
      "s_add_u32 s46, %[wb0], s40\n"
      "s_addc_u32 s47, %[wb1], 0\n"
      "global_load_lds_dwordx4 %[wv], s[46:47]\n"
      "s_add_u32 m0, m0, 0x400\n"
      "s_add_u32 s46, s46, 15360\n"
      "s_addc_u32 s47, s47, 0\n"
      "global_load_lds_dwordx4 %[wv], s[46:47]\n"
      "s_add_u32 s40, s40, 122880\n"
      "s_cmp_eq_u32 s40, 2457600\n"
      "s_cselect_b32 s40, 0, s40\n"
      "s_waitcnt lgkmcnt(3)\n"
      "v_mfma_f32_32x32x16_bf16 v[64:79], v[136:139], v[144:147], v[64:79]\n"
      "ds_read_b128 v[152:155], v171\n"
      "ds_read_b64_tr_b16 v[160:161], %[wa0] offset:12288\n"
      "ds_read_b64_tr_b16 v[162:163], %[wa0] offset:13312\n"
      "s_waitcnt lgkmcnt(4)\n"
      "v_mfma_f32_32x32x16_bf16 v[80:95], v[136:139], v[148:151], v[80:95]\n"
      "s_add_u32 m0, s45, 0\n"
      "s_add_u32 s43, s42, 0\n"
      "s_add_u32 s46, %[xb0], s43\n"
      "s_addc_u32 s47, %[xb1], 0\n"
      "global_load_lds_dwordx4 %[xe], s[46:47]\n"
      "s_add_u32 m0, m0, 0x400\n"
      "s_add_u32 s46, s46, 10240\n"
      "s_addc_u32 s47, s47, 0\n"
      "global_load_lds_dwordx4 %[xo], s[46:47]\n"
      "s_waitcnt lgkmcnt(3)\n"
      "v_mfma_f32_32x32x16_bf16 v[112:127], v[140:143], v[148:151], v[112:127]\n"
      "ds_read_b64_tr_b16 v[164:165], %[wa1] offset:12288\n"
      "ds_read_b64_tr_b16 v[166:167], %[wa1] offset:13312\n"
      "ds_read_b128 v[156:159], v175\n"
      "v_mfma_f32_32x32x16_bf16 v[96:111], v[140:143], v[144:147], v[96:111]\n"
      "s_add_u32 m0, s45, 2048\n"
      "s_add_u32 s43, s42, 20480\n"
      "s_add_u32 s46, %[xb0], s43\n"
      "s_addc_u32 s47, %[xb1], 0\n"
      "global_load_lds_dwordx4 %[xe], s[46:47]\n"
      "s_add_u32 m0, m0, 0x400\n"
      "s_add_u32 s46, s46, 10240\n"
      "s_addc_u32 s47, s47, 0\n"
      "global_load_lds_dwordx4 %[xo], s[46:47]\n"
      "s_add_u32 s42, s42, 128\n"
      "s_cmp_eq_u32 s42, 1280\n"
      "s_cselect_b32 s42, 0, s42\n"
      "s_add_u32 s45, s45, 16384\n"
      "s_cmp_eq_u32 s45, %[ldxe]\n"
      "s_cselect_b32 s45, %[ldx], s45\n"
      "v_mfma_f32_16x16x32_bf16 v[128:131], %[sel], v[144:147], v[128:131]\n"
      "s_add_u32 s44, s44, 16384\n"
      "s_cmp_eq_u32 s44, 49152\n"
      "s_cselect_b32 s44, 0, s44\n"
      "s_waitcnt vmcnt(6) lgkmcnt(0)\n"
      "s_barrier\n"
      "v_mfma_f32_16x16x32_bf16 v[128:131], %[sel], v[160:163], v[128:131]\n"
      "v_mfma_f32_32x32x16_bf16 v[64:79], v[152:155], v[160:163], v[64:79]\n"
      "v_add_u32 v168, s44, %[xa0]\n"
      "v_xor_b32 v172, 0x1000, v168\n"
      "ds_read_b128 v[136:139], v168\n"
      "ds_read_b64_tr_b16 v[144:145], %[wa0] offset:16384\n"
      "ds_read_b64_tr_b16 v[146:147], %[wa0] offset:17408\n"
      "v_mfma_f32_32x32x16_bf16 v[80:95], v[152:155], v[164:167], v[80:95]\n"
      "s_add_u32 m0, %[ldw], 0\n"
      "s_add_u32 s46, %[wb0], s40\n"
      "s_addc_u32 s47, %[wb1], 0\n"
      "global_load_lds_dwordx4 %[wv], s[46:47]\n"
      "s_add_u32 m0, m0, 0x400\n"
      "s_add_u32 s46, s46, 15360\n"
      "s_addc_u32 s47, s47, 0\n"
      "global_load_lds_dwordx4 %[wv], s[46:47]\n"
      "s_add_u32 s40, s40, 122880\n"
      "s_cmp_eq_u32 s40, 2457600\n"
      "s_cselect_b32 s40, 0, s40\n"
      "v_add_u32 v169, s44, %[xa1]\n"
      "v_add_u32 v170, s44, %[xa2]\n"
      "v_add_u32 v171, s44, %[xa3]\n"
      "v_mfma_f32_32x32x16_bf16 v[112:127], v[156:159], v[164:167], v[112:127]\n"
      "ds_read_b64_tr_b16 v[148:149], %[wa1] offset:16384\n"
      "ds_read_b64_tr_b16 v[150:151], %[wa1] offset:17408\n"
      "ds_read_b128 v[140:143], v172\n"
      "v_mfma_f32_32x32x16_bf16 v[96:111], v[156:159], v[160:163], v[96:111]\n"
      "s_add_u32 m0, %[ldw], 8192\n"
      "s_add_u32 s46, %[wb0], s40\n"
      "s_addc_u32 s47, %[wb1], 0\n"
      "global_load_lds_dwordx4 %[wv], s[46:47]\n"
      "s_add_u32 m0, m0, 0x400\n"
      "s_add_u32 s46, s46, 15360\n"
      "s_addc_u32 s47, s47, 0\n"
      "global_load_lds_dwordx4 %[wv], s[46:47]\n"
      "s_add_u32 s40, s40, 122880\n"
      "s_cmp_eq_u32 s40, 2457600\n"
      "s_cselect_b32 s40, 0, s40\n"
      "v_xor_b32 v173, 0x1000, v169\n"
      "v_xor_b32 v174, 0x1000, v170\n"
      "v_xor_b32 v175, 0x1000, v171\n"
      "s_waitcnt lgkmcnt(3)\n"
      "v_mfma_f32_32x32x16_bf16 v[64:79], v[136:139], v[144:147], v[64:79]\n"
      "ds_read_b128 v[152:155], v169\n"
      "ds_read_b64_tr_b16 v[160:161], %[wa0] offset:20480\n"
      "ds_read_b64_tr_b16 v[162:163], %[wa0] offset:21504\n"
      "s_waitcnt lgkmcnt(4)\n"
      "v_mfma_f32_32x32x16_bf16 v[80:95], v[136:139], v[148:151], v[80:95]\n"
      "s_add_u32 m0, s45, 0\n"
      "s_add_u32 s43, s42, 0\n"
      "s_add_u32 s46, %[xb0], s43\n"
      "s_addc_u32 s47, %[xb1], 0\n"
      "global_load_lds_dwordx4 %[xe], s[46:47]\n"
      "s_waitcnt lgkmcnt(3)\n"
      "v_mfma_f32_32x32x16_bf16 v[112:127], v[140:143], v[148:151], v[112:127]\n"
      "ds_read_b64_tr_b16 v[164:165], %[wa1] offset:20480\n"
      "ds_read_b64_tr_b16 v[166:167], %[wa1] offset:21504\n"
      "ds_read_b128 v[156:159], v173\n"
      "v_mfma_f32_32x32x16_bf16 v[96:111], v[140:143], v[144:147], v[96:111]\n"
      "s_add_u32 m0, m0, 0x400\n"
      "s_add_u32 s46, s46, 10240\n"
      "s_addc_u32 s47, s47, 0\n"
      "global_load_lds_dwordx4 %[xo], s[46:47]\n"
      "v_mfma_f32_16x16x32_bf16 v[128:131], %[sel], v[144:147], v[128:131]\n"
      "s_waitcnt vmcnt(10) lgkmcnt(0)\n"
      "s_barrier\n"
      "v_mfma_f32_16x16x32_bf16 v[128:131], %[sel], v[160:163], v[128:131]\n"
      "v_mfma_f32_32x32x16_bf16 v[64:79], v[152:155], v[160:163], v[64:79]\n"
      "ds_read_b128 v[136:139], v170\n"
      "ds_read_b64_tr_b16 v[144:145], %[wa0] offset:24576\n"
      "ds_read_b64_tr_b16 v[146:147], %[wa0] offset:25600\n"
      "v_mfma_f32_32x32x16_bf16 v[80:95], v[152:155], v[164:167], v[80:95]\n"
      "s_add_u32 m0, %[ldw], 16384\n"
      "s_add_u32 s46, %[wb0], s40\n"
      "s_addc_u32 s47, %[wb1], 0\n"
      "global_load_lds_dwordx4 %[wv], s[46:47]\n"
      "v_mfma_f32_32x32x16_bf16 v[112:127], v[156:159], v[164:167], v[112:127]\n"
      "ds_read_b64_tr_b16 v[148:149], %[wa1] offset:24576\n"
      "ds_read_b64_tr_b16 v[150:151], %[wa1] offset:25600\n"
      "ds_read_b128 v[140:143], v174\n"
      "v_mfma_f32_32x32x16_bf16 v[96:111], v[156:159], v[160:163], v[96:111]\n"
      "s_add_u32 m0, m0, 0x400\n"
      "s_add_u32 s46, s46, 15360\n"
      "s_addc_u32 s47, s47, 0\n"
      "global_load_lds_dwordx4 %[wv], s[46:47]\n"
      "s_add_u32 s40, s40, 122880\n"
      "s_cmp_eq_u32 s40, 2457600\n"
      "s_cselect_b32 s40, 0, s40\n"
      "s_waitcnt lgkmcnt(3)\n"
      "v_mfma_f32_32x32x16_bf16 v[64:79], v[136:139], v[144:147], v[64:79]\n"
      "ds_read_b128 v[152:155], v171\n"
      "ds_read_b64_tr_b16 v[160:161], %[wa0] offset:28672\n"
      "ds_read_b64_tr_b16 v[162:163], %[wa0] offset:29696\n"
      "s_waitcnt lgkmcnt(4)\n"
      "v_mfma_f32_32x32x16_bf16 v[80:95], v[136:139], v[148:151], v[80:95]\n"
      "s_add_u32 m0, s45, 2048\n"
      "s_add_u32 s43, s42, 20480\n"
      "s_add_u32 s46, %[xb0], s43\n"
      "s_addc_u32 s47, %[xb1], 0\n"
      "global_load_lds_dwordx4 %[xe], s[46:47]\n"
      "s_waitcnt lgkmcnt(3)\n"
      "v_mfma_f32_32x32x16_bf16 v[112:127], v[140:143], v[148:151], v[112:127]\n"
      "ds_read_b64_tr_b16 v[164:165], %[wa1] offset:28672\n"
      "ds_read_b64_tr_b16 v[166:167], %[wa1] offset:29696\n"
      "ds_read_b128 v[156:159], v175\n"
      "v_mfma_f32_32x32x16_bf16 v[96:111], v[140:143], v[144:147], v[96:111]\n"
      "s_add_u32 m0, m0, 0x400\n"
      "s_add_u32 s46, s46, 10240\n"
      "s_addc_u32 s47, s47, 0\n"
      "global_load_lds_dwordx4 %[xo], s[46:47]\n"
      "s_add_u32 s42, s42, 128\n"
      "s_cmp_eq_u32 s42, 1280\n"
      "s_cselect_b32 s42, 0, s42\n"
      "s_add_u32 s45, s45, 16384\n"
      "s_cmp_eq_u32 s45, %[ldxe]\n"
      "s_cselect_b32 s45, %[ldx], s45\n"
      "v_mfma_f32_16x16x32_bf16 v[128:131], %[sel], v[144:147], v[128:131]\n"
      "s_add_u32 s44, s44, 16384\n"
      "s_cmp_eq_u32 s44, 49152\n"
      "s_cselect_b32 s44, 0, s44\n"
      "s_waitcnt vmcnt(8) lgkmcnt(0)\n"
      "s_barrier\n"
      "v_mfma_f32_16x16x32_bf16 v[128:131], %[sel], v[160:163], v[128:131]\n"
      "v_mfma_f32_32x32x16_bf16 v[64:79], v[152:155], v[160:163], v[64:79]\n"
      "v_add_u32 v168, s44, %[xa0]\n"
      "v_xor_b32 v172, 0x1000, v168\n"
      "ds_read_b128 v[136:139], v168\n"
      "ds_read_b64_tr_b16 v[144:145], %[wa0] offset:0\n"
      "ds_read_b64_tr_b16 v[146:147], %[wa0] offset:1024\n"
      "v_mfma_f32_32x32x16_bf16 v[80:95], v[152:155], v[164:167], v[80:95]\n"
      "s_add_u32 m0, %[ldw], 24576\n"
      "s_add_u32 s46, %[wb0], s40\n"
      "s_addc_u32 s47, %[wb1], 0\n"
      "global_load_lds_dwordx4 %[wv], s[46:47]\n"
      "v_add_u32 v169, s44, %[xa1]\n"
      "v_add_u32 v170, s44, %[xa2]\n"
      "v_add_u32 v171, s44, %[xa3]\n"
      "v_mfma_f32_32x32x16_bf16 v[112:127], v[156:159], v[164:167], v[112:127]\n"
      "ds_read_b64_tr_b16 v[148:149], %[wa1] offset:0\n"
      "ds_read_b64_tr_b16 v[150:151], %[wa1] offset:1024\n"
      "ds_read_b128 v[140:143], v172\n"
      "v_mfma_f32_32x32x16_bf16 v[96:111], v[156:159], v[160:163], v[96:111]\n"
      "s_add_u32 m0, m0, 0x400\n"
      "s_add_u32 s46, s46, 15360\n"
      "s_addc_u32 s47, s47, 0\n"
      "global_load_lds_dwordx4 %[wv], s[46:47]\n"
      "s_add_u32 s40, s40, 122880\n"
      "s_cmp_eq_u32 s40, 2457600\n"
      "s_cselect_b32 s40, 0, s40\n"
      "v_xor_b32 v173, 0x1000, v169\n"
      "v_xor_b32 v174, 0x1000, v170\n"
      "v_xor_b32 v175, 0x1000, v171\n"
      ".p2align 3\n"
      "LOOP_%=:\n"
      "s_waitcnt lgkmcnt(3)\n"
      "v_mfma_f32_32x32x16_bf16 v[64:79], v[136:139], v[144:147], v[64:79]\n"
      "ds_read_b128 v[152:155], v169\n"
      "ds_read_b64_tr_b16 v[160:161], %[wa0] offset:4096\n"
      "ds_read_b64_tr_b16 v[162:163], %[wa0] offset:5120\n"
      "s_waitcnt lgkmcnt(4)\n"
      "v_mfma_f32_32x32x16_bf16 v[80:95], v[136:139], v[148:151], v[80:95]\n"
      "s_add_u32 m0, s45, 0\n"
      "s_add_u32 s43, s42, 0\n"
      "s_add_u32 s46, %[xb0], s43\n"
      "s_addc_u32 s47, %[xb1], 0\n"
      "global_load_lds_dwordx4 %[xe], s[46:47]\n"
      "s_waitcnt lgkmcnt(3)\n"
      "v_mfma_f32_32x32x16_bf16 v[112:127], v[140:143], v[148:151], v[112:127]\n"
      "ds_read_b64_tr_b16 v[164:165], %[wa1] offset:4096\n"
      "ds_read_b64_tr_b16 v[166:167], %[wa1] offset:5120\n"
      "ds_read_b128 v[156:159], v173\n"
      "v_mfma_f32_32x32x16_bf16 v[96:111], v[140:143], v[144:147], v[96:111]\n"
      "s_add_u32 m0, m0, 0x400\n"
      "s_add_u32 s46, s46, 10240\n"
      "s_addc_u32 s47, s47, 0\n"
      "global_load_lds_dwordx4 %[xo], s[46:47]\n"
      "v_mfma_f32_16x16x32_bf16 v[128:131], %[sel], v[144:147], v[128:131]\n"
      "s_waitcnt vmcnt(8) lgkmcnt(0)\n"
      "s_barrier\n"
      "v_mfma_f32_16x16x32_bf16 v[128:131], %[sel], v[160:163], v[128:131]\n"
      "v_mfma_f32_32x32x16_bf16 v[64:79], v[152:155], v[160:163], v[64:79]\n"
      "ds_read_b128 v[136:139], v170\n"
      "ds_read_b64_tr_b16 v[144:145], %[wa0] offset:8192\n"
      "ds_read_b64_tr_b16 v[146:147], %[wa0] offset:9216\n"
      "v_mfma_f32_32x32x16_bf16 v[80:95], v[152:155], v[164:167], v[80:95]\n"
      "s_add_u32 m0, %[ldw], 0\n"
      "s_add_u32 s46, %[wb0], s40\n"
      "s_addc_u32 s47, %[wb1], 0\n"
      "global_load_lds_dwordx4 %[wv], s[46:47]\n"
      "v_mfma_f32_32x32x16_bf16 v[112:127], v[156:159], v[164:167], v[112:127]\n"
      "ds_read_b64_tr_b16 v[148:149], %[wa1] offset:8192\n"
      "ds_read_b64_tr_b16 v[150:151], %[wa1] offset:9216\n"
      "ds_read_b128 v[140:143], v174\n"
      "v_mfma_f32_32x32x16_bf16 v[96:111], v[156:159], v[160:163], v[96:111]\n"
      "s_add_u32 m0, m0, 0x400\n"
      "s_add_u32 s46, s46, 15360\n"
      "s_addc_u32 s47, s47, 0\n"
      "global_load_lds_dwordx4 %[wv], s[46:47]\n"
      "s_add_u32 s40, s40, 122880\n"
      "s_cmp_eq_u32 s40, 2457600\n"
      "s_cselect_b32 s40, 0, s40\n"
      "s_waitcnt lgkmcnt(3)\n"
      "v_mfma_f32_32x32x16_bf16 v[64:79], v[136:139], v[144:147], v[64:79]\n"
      "ds_read_b128 v[152:155], v171\n"
      "ds_read_b64_tr_b16 v[160:161], %[wa0] offset:12288\n"
      "ds_read_b64_tr_b16 v[162:163], %[wa0] offset:13312\n"
      "s_waitcnt lgkmcnt(4)\n"
      "v_mfma_f32_32x32x16_bf16 v[80:95], v[136:139], v[148:151], v[80:95]\n"
      "s_add_u32 m0, s45, 2048\n"
      "s_add_u32 s43, s42, 20480\n"
      "s_add_u32 s46, %[xb0], s43\n"
      "s_addc_u32 s47, %[xb1], 0\n"
      "global_load_lds_dwordx4 %[xe], s[46:47]\n"
      "s_waitcnt lgkmcnt(3)\n"
      "v_mfma_f32_32x32x16_bf16 v[112:127], v[140:143], v[148:151], v[112:127]\n"
      "ds_read_b64_tr_b16 v[164:165], %[wa1] offset:12288\n"
      "ds_read_b64_tr_b16 v[166:167], %[wa1] offset:13312\n"
      "ds_read_b128 v[156:159], v175\n"
      "v_mfma_f32_32x32x16_bf16 v[96:111], v[140:143], v[144:147], v[96:111]\n"
      "s_add_u32 m0, m0, 0x400\n"
      "s_add_u32 s46, s46, 10240\n"
      "s_addc_u32 s47, s47, 0\n"
      "global_load_lds_dwordx4 %[xo], s[46:47]\n"
      "s_add_u32 s42, s42, 128\n"
      "s_cmp_eq_u32 s42, 1280\n"
      "s_cselect_b32 s42, 0, s42\n"
      "s_add_u32 s45, s45, 16384\n"
      "s_cmp_eq_u32 s45, %[ldxe]\n"
      "s_cselect_b32 s45, %[ldx], s45\n"
      "v_mfma_f32_16x16x32_bf16 v[128:131], %[sel], v[144:147], v[128:131]\n"
      "s_add_u32 s44, s44, 16384\n"
      "s_cmp_eq_u32 s44, 49152\n"
      "s_cselect_b32 s44, 0, s44\n"
      "s_waitcnt vmcnt(8) lgkmcnt(0)\n"
      "s_barrier\n"
      "v_mfma_f32_16x16x32_bf16 v[128:131], %[sel], v[160:163], v[128:131]\n"
      "v_mfma_f32_32x32x16_bf16 v[64:79], v[152:155], v[160:163], v[64:79]\n"
      "v_add_u32 v168, s44, %[xa0]\n"
      "v_xor_b32 v172, 0x1000, v168\n"
      "ds_read_b128 v[136:139], v168\n"
      "ds_read_b64_tr_b16 v[144:145], %[wa0] offset:16384\n"
      "ds_read_b64_tr_b16 v[146:147], %[wa0] offset:17408\n"
      "v_mfma_f32_32x32x16_bf16 v[80:95], v[152:155], v[164:167], v[80:95]\n"
      "s_add_u32 m0, %[ldw], 8192\n"
      "s_add_u32 s46, %[wb0], s40\n"
      "s_addc_u32 s47, %[wb1], 0\n"
      "global_load_lds_dwordx4 %[wv], s[46:47]\n"
      "v_add_u32 v169, s44, %[xa1]\n"
      "v_add_u32 v170, s44, %[xa2]\n"
      "v_add_u32 v171, s44, %[xa3]\n"
      "v_mfma_f32_32x32x16_bf16 v[112:127], v[156:159], v[164:167], v[112:127]\n"
      "ds_read_b64_tr_b16 v[148:149], %[wa1] offset:16384\n"
      "ds_read_b64_tr_b16 v[150:151], %[wa1] offset:17408\n"
      "ds_read_b128 v[140:143], v172\n"
      "v_mfma_f32_32x32x16_bf16 v[96:111], v[156:159], v[160:163], v[96:111]\n"
      "s_add_u32 m0, m0, 0x400\n"
      "s_add_u32 s46, s46, 15360\n"
      "s_addc_u32 s47, s47, 0\n"
      "global_load_lds_dwordx4 %[wv], s[46:47]\n"
      "s_add_u32 s40, s40, 122880\n"
      "s_cmp_eq_u32 s40, 2457600\n"
      "s_cselect_b32 s40, 0, s40\n"
      "v_xor_b32 v173, 0x1000, v169\n"
      "v_xor_b32 v174, 0x1000, v170\n"
      "v_xor_b32 v175, 0x1000, v171\n"
      "s_waitcnt lgkmcnt(3)\n"
      "v_mfma_f32_32x32x16_bf16 v[64:79], v[136:139], v[144:147], v[64:79]\n"
      "ds_read_b128 v[152:155], v169\n"
      "ds_read_b64_tr_b16 v[160:161], %[wa0] offset:20480\n"
      "ds_read_b64_tr_b16 v[162:163], %[wa0] offset:21504\n"
      "s_waitcnt lgkmcnt(4)\n"
      "v_mfma_f32_32x32x16_bf16 v[80:95], v[136:139], v[148:151], v[80:95]\n"
      "s_add_u32 m0, s45, 0\n"
      "s_add_u32 s43, s42, 0\n"
      "s_add_u32 s46, %[xb0], s43\n"
      "s_addc_u32 s47, %[xb1], 0\n"
      "global_load_lds_dwordx4 %[xe], s[46:47]\n"
      "s_waitcnt lgkmcnt(3)\n"
      "v_mfma_f32_32x32x16_bf16 v[112:127], v[140:143], v[148:151], v[112:127]\n"
      "ds_read_b64_tr_b16 v[164:165], %[wa1] offset:20480\n"
      "ds_read_b64_tr_b16 v[166:167], %[wa1] offset:21504\n"
      "ds_read_b128 v[156:159], v173\n"
      "v_mfma_f32_32x32x16_bf16 v[96:111], v[140:143], v[144:147], v[96:111]\n"
      "s_add_u32 m0, m0, 0x400\n"
      "s_add_u32 s46, s46, 10240\n"
      "s_addc_u32 s47, s47, 0\n"
      "global_load_lds_dwordx4 %[xo], s[46:47]\n"
      "v_mfma_f32_16x16x32_bf16 v[128:131], %[sel], v[144:147], v[128:131]\n"
      "s_waitcnt vmcnt(8) lgkmcnt(0)\n"
      "s_barrier\n"
      "v_mfma_f32_16x16x32_bf16 v[128:131], %[sel], v[160:163], v[128:131]\n"
      "v_mfma_f32_32x32x16_bf16 v[64:79], v[152:155], v[160:163], v[64:79]\n"
      "ds_read_b128 v[136:139], v170\n"
      "ds_read_b64_tr_b16 v[144:145], %[wa0] offset:24576\n"
      "ds_read_b64_tr_b16 v[146:147], %[wa0] offset:25600\n"
      "v_mfma_f32_32x32x16_bf16 v[80:95], v[152:155], v[164:167], v[80:95]\n"
      "s_add_u32 m0, %[ldw], 16384\n"
      "s_add_u32 s46, %[wb0], s40\n"
      "s_addc_u32 s47, %[wb1], 0\n"
      "global_load_lds_dwordx4 %[wv], s[46:47]\n"
      "v_mfma_f32_32x32x16_bf16 v[112:127], v[156:159], v[164:167], v[112:127]\n"
      "ds_read_b64_tr_b16 v[148:149], %[wa1] offset:24576\n"
      "ds_read_b64_tr_b16 v[150:151], %[wa1] offset:25600\n"
      "ds_read_b128 v[140:143], v174\n"
      "v_mfma_f32_32x32x16_bf16 v[96:111], v[156:159], v[160:163], v[96:111]\n"
      "s_add_u32 m0, m0, 0x400\n"
      "s_add_u32 s46, s46, 15360\n"
      "s_addc_u32 s47, s47, 0\n"
      "global_load_lds_dwordx4 %[wv], s[46:47]\n"
      "s_add_u32 s40, s40, 122880\n"
      "s_cmp_eq_u32 s40, 2457600\n"
      "s_cselect_b32 s40, 0, s40\n"
      "s_waitcnt lgkmcnt(3)\n"
      "v_mfma_f32_32x32x16_bf16 v[64:79], v[136:139], v[144:147], v[64:79]\n"
      "ds_read_b128 v[152:155], v171\n"
      "ds_read_b64_tr_b16 v[160:161], %[wa0] offset:28672\n"
      "ds_read_b64_tr_b16 v[162:163], %[wa0] offset:29696\n"
      "s_waitcnt lgkmcnt(4)\n"
      "v_mfma_f32_32x32x16_bf16 v[80:95], v[136:139], v[148:151], v[80:95]\n"
      "s_add_u32 m0, s45, 2048\n"
      "s_add_u32 s43, s42, 20480\n"
      "s_add_u32 s46, %[xb0], s43\n"
      "s_addc_u32 s47, %[xb1], 0\n"
      "global_load_lds_dwordx4 %[xe], s[46:47]\n"
      "s_waitcnt lgkmcnt(3)\n"
      "v_mfma_f32_32x32x16_bf16 v[112:127], v[140:143], v[148:151], v[112:127]\n"
      "ds_read_b64_tr_b16 v[164:165], %[wa1] offset:28672\n"
      "ds_read_b64_tr_b16 v[166:167], %[wa1] offset:29696\n"
      "ds_read_b128 v[156:159], v175\n"
      "v_mfma_f32_32x32x16_bf16 v[96:111], v[140:143], v[144:147], v[96:111]\n"
      "s_add_u32 m0, m0, 0x400\n"
      "s_add_u32 s46, s46, 10240\n"
      "s_addc_u32 s47, s47, 0\n"
      "global_load_lds_dwordx4 %[xo], s[46:47]\n"
      "s_add_u32 s42, s42, 128\n"
      "s_cmp_eq_u32 s42, 1280\n"
      "s_cselect_b32 s42, 0, s42\n"
      "s_add_u32 s45, s45, 16384\n"
      "s_cmp_eq_u32 s45, %[ldxe]\n"
      "s_cselect_b32 s45, %[ldx], s45\n"
      "v_mfma_f32_16x16x32_bf16 v[128:131], %[sel], v[144:147], v[128:131]\n"
      "s_add_u32 s44, s44, 16384\n"
      "s_cmp_eq_u32 s44, 49152\n"
      "s_cselect_b32 s44, 0, s44\n"
      "s_waitcnt vmcnt(8) lgkmcnt(0)\n"
      "s_barrier\n"
      "v_mfma_f32_16x16x32_bf16 v[128:131], %[sel], v[160:163], v[128:131]\n"
      "v_mfma_f32_32x32x16_bf16 v[64:79], v[152:155], v[160:163], v[64:79]\n"
      "v_add_u32 v168, s44, %[xa0]\n"
      "v_xor_b32 v172, 0x1000, v168\n"
      "ds_read_b128 v[136:139], v168\n"
      "ds_read_b64_tr_b16 v[144:145], %[wa0] offset:0\n"
      "ds_read_b64_tr_b16 v[146:147], %[wa0] offset:1024\n"
      "v_mfma_f32_32x32x16_bf16 v[80:95], v[152:155], v[164:167], v[80:95]\n"
      "s_add_u32 m0, %[ldw], 24576\n"
      "s_add_u32 s46, %[wb0], s40\n"
      "s_addc_u32 s47, %[wb1], 0\n"
      "global_load_lds_dwordx4 %[wv], s[46:47]\n"
      "v_add_u32 v169, s44, %[xa1]\n"
      "v_add_u32 v170, s44, %[xa2]\n"
      "v_add_u32 v171, s44, %[xa3]\n"
      "v_mfma_f32_32x32x16_bf16 v[112:127], v[156:159], v[164:167], v[112:127]\n"
      "ds_read_b64_tr_b16 v[148:149], %[wa1] offset:0\n"
      "ds_read_b64_tr_b16 v[150:151], %[wa1] offset:1024\n"
      "ds_read_b128 v[140:143], v172\n"
      "v_mfma_f32_32x32x16_bf16 v[96:111], v[156:159], v[160:163], v[96:111]\n"
      "s_add_u32 m0, m0, 0x400\n"
      "s_add_u32 s46, s46, 15360\n"
      "s_addc_u32 s47, s47, 0\n"
      "global_load_lds_dwordx4 %[wv], s[46:47]\n"
      "s_add_u32 s40, s40, 122880\n"
      "s_cmp_eq_u32 s40, 2457600\n"
      "s_cselect_b32 s40, 0, s40\n"
      "v_xor_b32 v173, 0x1000, v169\n"
      "v_xor_b32 v174, 0x1000, v170\n"
      "v_xor_b32 v175, 0x1000, v171\n"
      "s_sub_u32 s36, s36, 1\n"
      "s_cmp_lg_u32 s36, 0\n"
      "s_cbranch_scc1 LOOP_%=\n"
      "s_waitcnt lgkmcnt(3)\n"
      "v_mfma_f32_32x32x16_bf16 v[64:79], v[136:139], v[144:147], v[64:79]\n"
      "ds_read_b128 v[152:155], v169\n"
      "ds_read_b64_tr_b16 v[160:161], %[wa0] offset:4096\n"
      "ds_read_b64_tr_b16 v[162:163], %[wa0] offset:5120\n"
      "s_waitcnt lgkmcnt(4)\n"
      "v_mfma_f32_32x32x16_bf16 v[80:95], v[136:139], v[148:151], v[80:95]\n"
      "s_waitcnt lgkmcnt(3)\n"
      "v_mfma_f32_32x32x16_bf16 v[112:127], v[140:143], v[148:151], v[112:127]\n"
      "ds_read_b64_tr_b16 v[164:165], %[wa1] offset:4096\n"
      "ds_read_b64_tr_b16 v[166:167], %[wa1] offset:5120\n"
      "ds_read_b128 v[156:159], v173\n"
      "v_mfma_f32_32x32x16_bf16 v[96:111], v[140:143], v[144:147], v[96:111]\n"
      "v_mfma_f32_16x16x32_bf16 v[128:131], %[sel], v[144:147], v[128:131]\n"
      "s_waitcnt vmcnt(6) lgkmcnt(0)\n"
      "s_barrier\n"
      "v_mfma_f32_16x16x32_bf16 v[128:131], %[sel], v[160:163], v[128:131]\n"
      "v_mfma_f32_32x32x16_bf16 v[64:79], v[152:155], v[160:163], v[64:79]\n"
      "ds_read_b128 v[136:139], v170\n"
      "ds_read_b64_tr_b16 v[144:145], %[wa0] offset:8192\n"
      "ds_read_b64_tr_b16 v[146:147], %[wa0] offset:9216\n"
      "v_mfma_f32_32x32x16_bf16 v[80:95], v[152:155], v[164:167], v[80:95]\n"
      "global_load_dwordx4 v[132:135], %[gv], %[gb] sc1\n"
      "v_mfma_f32_32x32x16_bf16 v[112:127], v[156:159], v[164:167], v[112:127]\n"
      "ds_read_b64_tr_b16 v[148:149], %[wa1] offset:8192\n"
      "ds_read_b64_tr_b16 v[150:151], %[wa1] offset:9216\n"
      "ds_read_b128 v[140:143], v174\n"
      "v_mfma_f32_32x32x16_bf16 v[96:111], v[156:159], v[160:163], v[96:111]\n"
      "v_mov_b32 v176, 0\n"
      "global_load_dwordx2 v[176:177], v176, %[ep] sc1\n"
      "s_waitcnt lgkmcnt(3)\n"
      "v_mfma_f32_32x32x16_bf16 v[64:79], v[136:139], v[144:147], v[64:79]\n"
      "ds_read_b128 v[152:155], v171\n"
      "ds_read_b64_tr_b16 v[160:161], %[wa0] offset:12288\n"
      "ds_read_b64_tr_b16 v[162:163], %[wa0] offset:13312\n"
      "s_waitcnt lgkmcnt(4)\n"
      "v_mfma_f32_32x32x16_bf16 v[80:95], v[136:139], v[148:151], v[80:95]\n"
      "s_waitcnt lgkmcnt(3)\n"
      "v_mfma_f32_32x32x16_bf16 v[112:127], v[140:143], v[148:151], v[112:127]\n"
      "ds_read_b64_tr_b16 v[164:165], %[wa1] offset:12288\n"
      "ds_read_b64_tr_b16 v[166:167], %[wa1] offset:13312\n"
      "ds_read_b128 v[156:159], v175\n"
      "v_mfma_f32_32x32x16_bf16 v[96:111], v[140:143], v[144:147], v[96:111]\n"
      "v_mfma_f32_16x16x32_bf16 v[128:131], %[sel], v[144:147], v[128:131]\n"
      "s_add_u32 s44, s44, 16384\n"
      "s_cmp_eq_u32 s44, 49152\n"
      "s_cselect_b32 s44, 0, s44\n"
      "s_waitcnt vmcnt(4) lgkmcnt(0)\n"
      "s_barrier\n"
      "v_mfma_f32_16x16x32_bf16 v[128:131], %[sel], v[160:163], v[128:131]\n"
      "v_mfma_f32_32x32x16_bf16 v[64:79], v[152:155], v[160:163], v[64:79]\n"
      "v_add_u32 v168, s44, %[xa0]\n"
      "v_xor_b32 v172, 0x1000, v168\n"
      "ds_read_b128 v[136:139], v168\n"
      "ds_read_b64_tr_b16 v[144:145], %[wa0] offset:16384\n"
      "ds_read_b64_tr_b16 v[146:147], %[wa0] offset:17408\n"
      "v_mfma_f32_32x32x16_bf16 v[80:95], v[152:155], v[164:167], v[80:95]\n"
      "v_add_u32 v169, s44, %[xa1]\n"
      "v_add_u32 v170, s44, %[xa2]\n"
      "v_add_u32 v171, s44, %[xa3]\n"
      "v_mfma_f32_32x32x16_bf16 v[112:127], v[156:159], v[164:167], v[112:127]\n"
      "ds_read_b64_tr_b16 v[148:149], %[wa1] offset:16384\n"
      "ds_read_b64_tr_b16 v[150:151], %[wa1] offset:17408\n"
      "ds_read_b128 v[140:143], v172\n"
      "v_mfma_f32_32x32x16_bf16 v[96:111], v[156:159], v[160:163], v[96:111]\n"
      "v_xor_b32 v173, 0x1000, v169\n"
      "v_xor_b32 v174, 0x1000, v170\n"
      "v_xor_b32 v175, 0x1000, v171\n"
      "s_waitcnt lgkmcnt(3)\n"
      "v_mfma_f32_32x32x16_bf16 v[64:79], v[136:139], v[144:147], v[64:79]\n"
      "ds_read_b128 v[152:155], v169\n"
      "ds_read_b64_tr_b16 v[160:161], %[wa0] offset:20480\n"
      "ds_read_b64_tr_b16 v[162:163], %[wa0] offset:21504\n"
      "s_waitcnt lgkmcnt(4)\n"
      "v_mfma_f32_32x32x16_bf16 v[80:95], v[136:139], v[148:151], v[80:95]\n"
      "s_waitcnt lgkmcnt(3)\n"
      "v_mfma_f32_32x32x16_bf16 v[112:127], v[140:143], v[148:151], v[112:127]\n"
      "ds_read_b64_tr_b16 v[164:165], %[wa1] offset:20480\n"
      "ds_read_b64_tr_b16 v[166:167], %[wa1] offset:21504\n"
      "ds_read_b128 v[156:159], v173\n"
      "v_mfma_f32_32x32x16_bf16 v[96:111], v[140:143], v[144:147], v[96:111]\n"
      "v_mfma_f32_16x16x32_bf16 v[128:131], %[sel], v[144:147], v[128:131]\n"
      "s_waitcnt vmcnt(0) lgkmcnt(0)\n"
      "s_barrier\n"
      "v_mfma_f32_16x16x32_bf16 v[128:131], %[sel], v[160:163], v[128:131]\n"
      "v_mfma_f32_32x32x16_bf16 v[64:79], v[152:155], v[160:163], v[64:79]\n"
      "ds_read_b128 v[136:139], v170\n"
      "ds_read_b64_tr_b16 v[144:145], %[wa0] offset:24576\n"
      "ds_read_b64_tr_b16 v[146:147], %[wa0] offset:25600\n"
      "v_mfma_f32_32x32x16_bf16 v[80:95], v[152:155], v[164:167], v[80:95]\n"
      "v_mfma_f32_32x32x16_bf16 v[112:127], v[156:159], v[164:167], v[112:127]\n"
      "ds_read_b64_tr_b16 v[148:149], %[wa1] offset:24576\n"
      "ds_read_b64_tr_b16 v[150:151], %[wa1] offset:25600\n"
      "ds_read_b128 v[140:143], v174\n"
      "v_mfma_f32_32x32x16_bf16 v[96:111], v[156:159], v[160:163], v[96:111]\n"
      "s_waitcnt lgkmcnt(3)\n"
      "v_mfma_f32_32x32x16_bf16 v[64:79], v[136:139], v[144:147], v[64:79]\n"
      "ds_read_b128 v[152:155], v171\n"
      "ds_read_b64_tr_b16 v[160:161], %[wa0] offset:28672\n"
      "ds_read_b64_tr_b16 v[162:163], %[wa0] offset:29696\n"
      "v_readfirstlane_b32 %[tag0], v176\n"
      "v_readfirstlane_b32 %[tag1], v177\n"
      "s_waitcnt lgkmcnt(4)\n"
      "v_mfma_f32_32x32x16_bf16 v[80:95], v[136:139], v[148:151], v[80:95]\n"
      "ds_read_b64_tr_b16 v[164:165], %[wa1] offset:28672\n"
      "ds_read_b64_tr_b16 v[166:167], %[wa1] offset:29696\n"
      "ds_read_b128 v[156:159], v175\n"
      "s_add_u32 %[tag0], %[tag0], 1\n"
      "s_addc_u32 %[tag1], %[tag1], 0\n"
      "s_waitcnt lgkmcnt(6)\n"
      "v_mfma_f32_32x32x16_bf16 v[112:127], v[140:143], v[148:151], v[112:127]\n"
      "v_lshlrev_b32 v168, 16, v133\n"
      "v_rcp_f32 v168, v168\n"
      "v_and_b32 v169, 0xffff0000, v135\n"
      "v_xor_b32 v169, 0x80000000, v169\n"
      "v_mul_f32 v168, %[qs], v168\n"
      "v_mfma_f32_32x32x16_bf16 v[96:111], v[140:143], v[144:147], v[96:111]\n"
      "ds_write_b64 %[sw], v[168:169]\n"
      "v_cmp_ne_u32 vcc, %[tag0], v132\n"
      "v_cmp_ne_u32 s[46:47], %[tag1], v134\n"
      "v_mfma_f32_16x16x32_bf16 v[128:131], %[sel], v[144:147], v[128:131]\n"
      "s_waitcnt lgkmcnt(4)\n"
      "v_mfma_f32_16x16x32_bf16 v[128:131], %[sel], v[160:163], v[128:131]\n"
      "v_mfma_f32_32x32x16_bf16 v[64:79], v[152:155], v[160:163], v[64:79]\n"
      "s_or_b64 s[46:47], s[46:47], vcc\n"
      "s_mov_b32 vcc_lo, 0xffff0000\n"
      "s_mov_b32 vcc_hi, 0xffff0000\n"
      "s_waitcnt lgkmcnt(2)\n"
      "v_mfma_f32_32x32x16_bf16 v[80:95], v[152:155], v[164:167], v[80:95]\n"
      "s_nop 2\n"
      "v_cndmask_b32 v170, v128, v129, vcc\n"
      "ds_write_b32 %[cw], v170\n"
      "s_waitcnt lgkmcnt(1)\n"
      "v_mfma_f32_32x32x16_bf16 v[112:127], v[156:159], v[164:167], v[112:127]\n"
      "v_mfma_f32_32x32x16_bf16 v[96:111], v[156:159], v[160:163], v[96:111]\n"
      "s_cmp_eq_u64 s[46:47], 0\n"
      "s_cbranch_scc1 LOK_%=\n"
      "s_mov_b32 s36, 0x10000\n"
      "LBAD_%=:\n"
      "s_sleep 4\n"
      "global_load_dwordx4 v[132:135], %[gv], %[gb] sc1\n"
      "s_waitcnt vmcnt(0)\n"
      "v_cmp_ne_u32 vcc, %[tag0], v132\n"
      "v_cmp_ne_u32 s[46:47], %[tag1], v134\n"
      "s_sub_u32 s36, s36, 1\n"
      "s_cbranch_scc1 LGIVE_%=\n"
      "s_or_b64 s[46:47], s[46:47], vcc\n"
      "s_cbranch_scc1 LBAD_%=\n"
      "LGIVE_%=:\n"
      "v_lshlrev_b32 v168, 16, v133\n"
      "v_rcp_f32 v168, v168\n"
      "v_and_b32 v169, 0xffff0000, v135\n"
      "v_xor_b32 v169, 0x80000000, v169\n"
      "v_mul_f32 v168, %[qs], v168\n"
      "ds_write_b64 %[sw], v[168:169]\n"
      "LOK_%=:\n"
      "s_waitcnt lgkmcnt(0)\n"
      "s_barrier\n"
      "s_nop 7\n"
      QKV_OPS : "v128", "v129", "v130", "v131", "v132", "v133", "v134", "v135", "v136", "v137", "v138", "v139", "v140", "v141", "v142", "v143", "v144", "v145", "v146", "v147", "v148", "v149", "v150", "v151", "v152", "v153", "v154", "v155", "v156", "v157", "v158", "v159", "v160", "v161", "v162", "v163", "v164", "v165", "v166", "v167", "v168", "v169", "v170", "v171", "v172", "v173", "v174", "v175", "v176", "v177", "s36", "s40", "s41", "s42", "s43", "s44", "s45", "s46", "s47", "scc", "memory", "vcc"
    );
  } else {
    asm volatile(
      "s_mul_i32 s40, %[k1], 245760\n"
      "s_lshl_b32 s42, %[k1], 7\n"
      "s_mov_b32 s44, 0\n"
      "s_add_u32 s45, %[ldx], 16384\n"
      "s_mov_b32 s36, 3\n"
      "v_add_u32 v168, s44, %[xa0]\n"
      "v_add_u32 v169, s44, %[xa1]\n"
      "v_add_u32 v170, s44, %[xa2]\n"
      "v_add_u32 v171, s44, %[xa3]\n"
      "v_xor_b32 v172, 0x1000, v168\n"
      "v_xor_b32 v173, 0x1000, v169\n"
      "v_xor_b32 v174, 0x1000, v170\n"
      "v_xor_b32 v175, 0x1000, v171\n"
      "v_mov_b32 v64, 0\n"
      "v_mov_b32 v65, 0\n"
      "v_mov_b32 v66, 0\n"
      "v_mov_b32 v67, 0\n"
      "v_mov_b32 v68, 0\n"
      "v_mov_b32 v69, 0\n"
      "v_mov_b32 v70, 0\n"
      "v_mov_b32 v71, 0\n"
      "v_mov_b32 v72, 0\n"
      "v_mov_b32 v73, 0\n"
      "v_mov_b32 v74, 0\n"
      "v_mov_b32 v75, 0\n"
      "v_mov_b32 v76, 0\n"
      "v_mov_b32 v77, 0\n"
      "v_mov_b32 v78, 0\n"
      "v_mov_b32 v79, 0\n"
      "v_mov_b32 v80, 0\n"
      "v_mov_b32 v81, 0\n"
      "v_mov_b32 v82, 0\n"
      "v_mov_b32 v83, 0\n"
      "v_mov_b32 v84, 0\n"
      "v_mov_b32 v85, 0\n"
      "v_mov_b32 v86, 0\n"
      "v_mov_b32 v87, 0\n"
      "v_mov_b32 v88, 0\n"
      "v_mov_b32 v89, 0\n"
      "v_mov_b32 v90, 0\n"
      "v_mov_b32 v91, 0\n"
      "v_mov_b32 v92, 0\n"
      "v_mov_b32 v93, 0\n"
      "v_mov_b32 v94, 0\n"
      "v_mov_b32 v95, 0\n"
      "v_mov_b32 v96, 0\n"
      "v_mov_b32 v97, 0\n"
      "v_mov_b32 v98, 0\n"
      "v_mov_b32 v99, 0\n"
      "v_mov_b32 v100, 0\n"
      "v_mov_b32 v101, 0\n"
      "v_mov_b32 v102, 0\n"
      "v_mov_b32 v103, 0\n"
      "v_mov_b32 v104, 0\n"
      "v_mov_b32 v105, 0\n"
      "v_mov_b32 v106, 0\n"
      "v_mov_b32 v107, 0\n"
      "v_mov_b32 v108, 0\n"
      "v_mov_b32 v109, 0\n"
      "v_mov_b32 v110, 0\n"
      "v_mov_b32 v111, 0\n"
      "v_mov_b32 v112, 0\n"
      "v_mov_b32 v113, 0\n"
      "v_mov_b32 v114, 0\n"
      "v_mov_b32 v115, 0\n"
      "v_mov_b32 v116, 0\n"
      "v_mov_b32 v117, 0\n"
      "v_mov_b32 v118, 0\n"
      "v_mov_b32 v119, 0\n"
      "v_mov_b32 v120, 0\n"
      "v_mov_b32 v121, 0\n"
      "v_mov_b32 v122, 0\n"
      "v_mov_b32 v123, 0\n"
      "v_mov_b32 v124, 0\n"
      "v_mov_b32 v125, 0\n"
      "v_mov_b32 v126, 0\n"
      "v_mov_b32 v127, 0\n"
      "v_mov_b32 v128, 0\n"
      "v_mov_b32 v129, 0\n"
      "v_mov_b32 v130, 0\n"
      "v_mov_b32 v131, 0\n"
      "s_waitcnt vmcnt(2)\n"
      "s_barrier\n"
      "ds_read_b128 v[136:139], v168\n"
      "ds_read_b64_tr_b16 v[144:145], %[wa0] offset:0\n"
      "ds_read_b64_tr_b16 v[146:147], %[wa0] offset:1024\n"
      "ds_read_b64_tr_b16 v[148:149], %[wa1] offset:0\n"
      "ds_read_b64_tr_b16 v[150:151], %[wa1] offset:1024\n"
      "ds_read_b128 v[140:143], v172\n"
      "s_waitcnt lgkmcnt(3)\n"
      "v_mfma_f32_32x32x16_bf16 v[64:79], v[144:147], v[136:139], v[64:79]\n"
      "ds_read_b128 v[152:155], v169\n"
      "ds_read_b64_tr_b16 v[160:161], %[wa0] offset:4096\n"
      "ds_read_b64_tr_b16 v[162:163], %[wa0] offset:5120\n"
      "s_waitcnt lgkmcnt(4)\n"
      "v_mfma_f32_32x32x16_bf16 v[80:95], v[148:151], v[136:139], v[80:95]\n"
      "s_add_u32 m0, s45, 0\n"
      "s_add_u32 s43, s42, 0\n"
      "s_add_u32 s46, %[xb0], s43\n"
      "s_addc_u32 s47, %[xb1], 0\n"
      "global_load_lds_dwordx4 %[xe], s[46:47]\n"
      "s_add_u32 m0, m0, 0x400\n"
      "s_add_u32 s46, s46, 10240\n"
      "s_addc_u32 s47, s47, 0\n"
      "global_load_lds_dwordx4 %[xo], s[46:47]\n"
      "s_waitcnt lgkmcnt(3)\n"
      "v_mfma_f32_32x32x16_bf16 v[112:127], v[148:151], v[140:143], v[112:127]\n"
      "ds_read_b64_tr_b16 v[164:165], %[wa1] offset:4096\n"
      "ds_read_b64_tr_b16 v[166:167], %[wa1] offset:5120\n"
      "ds_read_b128 v[156:159], v173\n"
      "v_mfma_f32_32x32x16_bf16 v[96:111], v[144:147], v[140:143], v[96:111]\n"
      "s_add_u32 m0, s45, 2048\n"
      "s_add_u32 s43, s42, 20480\n"
      "s_add_u32 s46, %[xb0], s43\n"
      "s_addc_u32 s47, %[xb1], 0\n"
      "global_load_lds_dwordx4 %[xe], s[46:47]\n"
      "s_add_u32 m0, m0, 0x400\n"
      "s_add_u32 s46, s46, 10240\n"
      "s_addc_u32 s47, s47, 0\n"
      "global_load_lds_dwordx4 %[xo], s[46:47]\n"
      "s_add_u32 s42, s42, 128\n"
      "s_cmp_eq_u32 s42, 1280\n"
      "s_cselect_b32 s42, 0, s42\n"
      "s_add_u32 s45, s45, 16384\n"
      "s_cmp_eq_u32 s45, %[ldxe]\n"
      "s_cselect_b32 s45, %[ldx], s45\n"
      "v_mfma_f32_16x16x32_bf16 v[128:131], %[sel], v[144:147], v[128:131]\n"
      "s_waitcnt vmcnt(4) lgkmcnt(0)\n"
      "s_barrier\n"
      "v_mfma_f32_16x16x32_bf16 v[128:131], %[sel], v[160:163], v[128:131]\n"
      "v_mfma_f32_32x32x16_bf16 v[64:79], v[160:163], v[152:155], v[64:79]\n"
      "ds_read_b128 v[136:139], v170\n"
      "ds_read_b64_tr_b16 v[144:145], %[wa0] offset:8192\n"
      "ds_read_b64_tr_b16 v[146:147], %[wa0] offset:9216\n"
      "v_mfma_f32_32x32x16_bf16 v[80:95], v[164:167], v[152:155], v[80:95]\n"
      "s_add_u32 m0, %[ldw], 16384\n"
      "s_add_u32 s46, %[wb0], s40\n"
      "s_addc_u32 s47, %[wb1], 0\n"
      "global_load_lds_dwordx4 %[wv], s[46:47]\n"
      "s_add_u32 m0, m0, 0x400\n"
      "s_add_u32 s46, s46, 15360\n"
      "s_addc_u32 s47, s47, 0\n"
      "global_load_lds_dwordx4 %[wv], s[46:47]\n"
      "s_add_u32 s40, s40, 122880\n"
      "s_cmp_eq_u32 s40, 2457600\n"
      "s_cselect_b32 s40, 0, s40\n"
      "v_mfma_f32_32x32x16_bf16 v[112:127], v[164:167], v[156:159], v[112:127]\n"
      "ds_read_b64_tr_b16 v[148:149], %[wa1] offset:8192\n"
      "ds_read_b64_tr_b16 v[150:151], %[wa1] offset:9216\n"
      "ds_read_b128 v[140:143], v174\n"
      "v_mfma_f32_32x32x16_bf16 v[96:111], v[160:163], v[156:159], v[96:111]\n"
      "s_add_u32 m0, %[ldw], 24576\n"
      "s_add_u32 s46, %[wb0], s40\n"
      "s_addc_u32 s47, %[wb1], 0\n"
      "global_load_lds_dwordx4 %[wv], s[46:47]\n"
      "s_add_u32 m0, m0, 0x400\n"
      "s_add_u32 s46, s46, 15360\n"
      "s_addc_u32 s47, s47, 0\n"
      "global_load_lds_dwordx4 %[wv], s[46:47]\n"
      "s_add_u32 s40, s40, 122880\n"
      "s_cmp_eq_u32 s40, 2457600\n"
      "s_cselect_b32 s40, 0, s40\n"
      "s_waitcnt lgkmcnt(3)\n"
      "v_mfma_f32_32x32x16_bf16 v[64:79], v[144:147], v[136:139], v[64:79]\n"
      "ds_read_b128 v[152:155], v171\n"
      "ds_read_b64_tr_b16 v[160:161], %[wa0] offset:12288\n"
      "ds_read_b64_tr_b16 v[162:163], %[wa0] offset:13312\n"
      "s_waitcnt lgkmcnt(4)\n"
      "v_mfma_f32_32x32x16_bf16 v[80:95], v[148:151], v[136:139], v[80:95]\n"
      "s_add_u32 m0, s45, 0\n"
      "s_add_u32 s43, s42, 0\n"
      "s_add_u32 s46, %[xb0], s43\n"
      "s_addc_u32 s47, %[xb1], 0\n"
      "global_load_lds_dwordx4 %[xe], s[46:47]\n"
      "s_add_u32 m0, m0, 0x400\n"
      "s_add_u32 s46, s46, 10240\n"
      "s_addc_u32 s47, s47, 0\n"
      "global_load_lds_dwordx4 %[xo], s[46:47]\n"
      "s_waitcnt lgkmcnt(3)\n"
      "v_mfma_f32_32x32x16_bf16 v[112:127], v[148:151], v[140:143], v[112:127]\n"
      "ds_read_b64_tr_b16 v[164:165], %[wa1] offset:12288\n"
      "ds_read_b64_tr_b16 v[166:167], %[wa1] offset:13312\n"
      "ds_read_b128 v[156:159], v175\n"
      "v_mfma_f32_32x32x16_bf16 v[96:111], v[144:147], v[140:143], v[96:111]\n"
      "s_add_u32 m0, s45, 2048\n"
      "s_add_u32 s43, s42, 20480\n"
      "s_add_u32 s46, %[xb0], s43\n"
      "s_addc_u32 s47, %[xb1], 0\n"
      "global_load_lds_dwordx4 %[xe], s[46:47]\n"
      "s_add_u32 m0, m0, 0x400\n"
      "s_add_u32 s46, s46, 10240\n"
      "s_addc_u32 s47, s47, 0\n"
      "global_load_lds_dwordx4 %[xo], s[46:47]\n"
      "s_add_u32 s42, s42, 128\n"
      "s_cmp_eq_u32 s42, 1280\n"
      "s_cselect_b32 s42, 0, s42\n"
      "s_add_u32 s45, s45, 16384\n"
      "s_cmp_eq_u32 s45, %[ldxe]\n"
      "s_cselect_b32 s45, %[ldx], s45\n"
      "v_mfma_f32_16x16x32_bf16 v[128:131], %[sel], v[144:147], v[128:131]\n"
      "s_add_u32 s44, s44, 16384\n"
      "s_cmp_eq_u32 s44, 49152\n"
      "s_cselect_b32 s44, 0, s44\n"
      "s_waitcnt vmcnt(6) lgkmcnt(0)\n"
      "s_barrier\n"
      "v_mfma_f32_16x16x32_bf16 v[128:131], %[sel], v[160:163], v[128:131]\n"
      "v_mfma_f32_32x32x16_bf16 v[64:79], v[160:163], v[152:155], v[64:79]\n"
      "v_add_u32 v168, s44, %[xa0]\n"
      "v_xor_b32 v172, 0x1000, v168\n"
      "ds_read_b128 v[136:139], v168\n"
      "ds_read_b64_tr_b16 v[144:145], %[wa0] offset:16384\n"
      "ds_read_b64_tr_b16 v[146:147], %[wa0] offset:17408\n"
      "v_mfma_f32_32x32x16_bf16 v[80:95], v[164:167], v[152:155], v[80:95]\n"
      "s_add_u32 m0, %[ldw], 0\n"
      "s_add_u32 s46, %[wb0], s40\n"
      "s_addc_u32 s47, %[wb1], 0\n"
      "global_load_lds_dwordx4 %[wv], s[46:47]\n"
      "s_add_u32 m0, m0, 0x400\n"
      "s_add_u32 s46, s46, 15360\n"
      "s_addc_u32 s47, s47, 0\n"
      "global_load_lds_dwordx4 %[wv], s[46:47]\n"
      "s_add_u32 s40, s40, 122880\n"
      "s_cmp_eq_u32 s40, 2457600\n"
      "s_cselect_b32 s40, 0, s40\n"
      "v_add_u32 v169, s44, %[xa1]\n"
      "v_add_u32 v170, s44, %[xa2]\n"
      "v_add_u32 v171, s44, %[xa3]\n"
      "v_mfma_f32_32x32x16_bf16 v[112:127], v[164:167], v[156:159], v[112:127]\n"
      "ds_read_b64_tr_b16 v[148:149], %[wa1] offset:16384\n"
      "ds_read_b64_tr_b16 v[150:151], %[wa1] offset:17408\n"
      "ds_read_b128 v[140:143], v172\n"
      "v_mfma_f32_32x32x16_bf16 v[96:111], v[160:163], v[156:159], v[96:111]\n"
      "s_add_u32 m0, %[ldw], 8192\n"
      "s_add_u32 s46, %[wb0], s40\n"
      "s_addc_u32 s47, %[wb1], 0\n"
      "global_load_lds_dwordx4 %[wv], s[46:47]\n"
      "s_add_u32 m0, m0, 0x400\n"
      "s_add_u32 s46, s46, 15360\n"
      "s_addc_u32 s47, s47, 0\n"
      "global_load_lds_dwordx4 %[wv], s[46:47]\n"
      "s_add_u32 s40, s40, 122880\n"
      "s_cmp_eq_u32 s40, 2457600\n"
      "s_cselect_b32 s40, 0, s40\n"
      "v_xor_b32 v173, 0x1000, v169\n"
      "v_xor_b32 v174, 0x1000, v170\n"
      "v_xor_b32 v175, 0x1000, v171\n"
      "s_waitcnt lgkmcnt(3)\n"
      "v_mfma_f32_32x32x16_bf16 v[64:79], v[144:147], v[136:139], v[64:79]\n"
      "ds_read_b128 v[152:155], v169\n"
      "ds_read_b64_tr_b16 v[160:161], %[wa0] offset:20480\n"
      "ds_read_b64_tr_b16 v[162:163], %[wa0] offset:21504\n"
      "s_waitcnt lgkmcnt(4)\n"
      "v_mfma_f32_32x32x16_bf16 v[80:95], v[148:151], v[136:139], v[80:95]\n"
      "s_add_u32 m0, s45, 0\n"
      "s_add_u32 s43, s42, 0\n"
      "s_add_u32 s46, %[xb0], s43\n"
      "s_addc_u32 s47, %[xb1], 0\n"
      "global_load_lds_dwordx4 %[xe], s[46:47]\n"
      "s_waitcnt lgkmcnt(3)\n"
      "v_mfma_f32_32x32x16_bf16 v[112:127], v[148:151], v[140:143], v[112:127]\n"
      "ds_read_b64_tr_b16 v[164:165], %[wa1] offset:20480\n"
      "ds_read_b64_tr_b16 v[166:167], %[wa1] offset:21504\n"
      "ds_read_b128 v[156:159], v173\n"
      "v_mfma_f32_32x32x16_bf16 v[96:111], v[144:147], v[140:143], v[96:111]\n"
      "s_add_u32 m0, m0, 0x400\n"
      "s_add_u32 s46, s46, 10240\n"
      "s_addc_u32 s47, s47, 0\n"
      "global_load_lds_dwordx4 %[xo], s[46:47]\n"
      "v_mfma_f32_16x16x32_bf16 v[128:131], %[sel], v[144:147], v[128:131]\n"
      "s_waitcnt vmcnt(10) lgkmcnt(0)\n"
      "s_barrier\n"
      "v_mfma_f32_16x16x32_bf16 v[128:131], %[sel], v[160:163], v[128:131]\n"
      "v_mfma_f32_32x32x16_bf16 v[64:79], v[160:163], v[152:155], v[64:79]\n"
      "ds_read_b128 v[136:139], v170\n"
      "ds_read_b64_tr_b16 v[144:145], %[wa0] offset:24576\n"
      "ds_read_b64_tr_b16 v[146:147], %[wa0] offset:25600\n"
      "v_mfma_f32_32x32x16_bf16 v[80:95], v[164:167], v[152:155], v[80:95]\n"
      "s_add_u32 m0, %[ldw], 16384\n"
      "s_add_u32 s46, %[wb0], s40\n"
      "s_addc_u32 s47, %[wb1], 0\n"
      "global_load_lds_dwordx4 %[wv], s[46:47]\n"
      "v_mfma_f32_32x32x16_bf16 v[112:127], v[164:167], v[156:159], v[112:127]\n"
      "ds_read_b64_tr_b16 v[148:149], %[wa1] offset:24576\n"
      "ds_read_b64_tr_b16 v[150:151], %[wa1] offset:25600\n"
      "ds_read_b128 v[140:143], v174\n"
      "v_mfma_f32_32x32x16_bf16 v[96:111], v[160:163], v[156:159], v[96:111]\n"
      "s_add_u32 m0, m0, 0x400\n"
      "s_add_u32 s46, s46, 15360\n"
      "s_addc_u32 s47, s47, 0\n"
      "global_load_lds_dwordx4 %[wv], s[46:47]\n"
      "s_add_u32 s40, s40, 122880\n"
      "s_cmp_eq_u32 s40, 2457600\n"
      "s_cselect_b32 s40, 0, s40\n"
      "s_waitcnt lgkmcnt(3)\n"
      "v_mfma_f32_32x32x16_bf16 v[64:79], v[144:147], v[136:139], v[64:79]\n"
      "ds_read_b128 v[152:155], v171\n"
      "ds_read_b64_tr_b16 v[160:161], %[wa0] offset:28672\n"
      "ds_read_b64_tr_b16 v[162:163], %[wa0] offset:29696\n"
      "s_waitcnt lgkmcnt(4)\n"
      "v_mfma_f32_32x32x16_bf16 v[80:95], v[148:151], v[136:139], v[80:95]\n"
      "s_add_u32 m0, s45, 2048\n"
      "s_add_u32 s43, s42, 20480\n"
      "s_add_u32 s46, %[xb0], s43\n"
      "s_addc_u32 s47, %[xb1], 0\n"
      "global_load_lds_dwordx4 %[xe], s[46:47]\n"
      "s_waitcnt lgkmcnt(3)\n"
      "v_mfma_f32_32x32x16_bf16 v[112:127], v[148:151], v[140:143], v[112:127]\n"
      "ds_read_b64_tr_b16 v[164:165], %[wa1] offset:28672\n"
      "ds_read_b64_tr_b16 v[166:167], %[wa1] offset:29696\n"
      "ds_read_b128 v[156:159], v175\n"
      "v_mfma_f32_32x32x16_bf16 v[96:111], v[144:147], v[140:143], v[96:111]\n"
      "s_add_u32 m0, m0, 0x400\n"
      "s_add_u32 s46, s46, 10240\n"
      "s_addc_u32 s47, s47, 0\n"
      "global_load_lds_dwordx4 %[xo], s[46:47]\n"
      "s_add_u32 s42, s42, 128\n"
      "s_cmp_eq_u32 s42, 1280\n"
      "s_cselect_b32 s42, 0, s42\n"
      "s_add_u32 s45, s45, 16384\n"
      "s_cmp_eq_u32 s45, %[ldxe]\n"
      "s_cselect_b32 s45, %[ldx], s45\n"
      "v_mfma_f32_16x16x32_bf16 v[128:131], %[sel], v[144:147], v[128:131]\n"
      "s_add_u32 s44, s44, 16384\n"
      "s_cmp_eq_u32 s44, 49152\n"
      "s_cselect_b32 s44, 0, s44\n"
      "s_waitcnt vmcnt(8) lgkmcnt(0)\n"
      "s_barrier\n"
      "v_mfma_f32_16x16x32_bf16 v[128:131], %[sel], v[160:163], v[128:131]\n"
      "v_mfma_f32_32x32x16_bf16 v[64:79], v[160:163], v[152:155], v[64:79]\n"
      "v_add_u32 v168, s44, %[xa0]\n"
      "v_xor_b32 v172, 0x1000, v168\n"
      "ds_read_b128 v[136:139], v168\n"
      "ds_read_b64_tr_b16 v[144:145], %[wa0] offset:0\n"
      "ds_read_b64_tr_b16 v[146:147], %[wa0] offset:1024\n"
      "v_mfma_f32_32x32x16_bf16 v[80:95], v[164:167], v[152:155], v[80:95]\n"
      "s_add_u32 m0, %[ldw], 24576\n"
      "s_add_u32 s46, %[wb0], s40\n"
      "s_addc_u32 s47, %[wb1], 0\n"
      "global_load_lds_dwordx4 %[wv], s[46:47]\n"
      "v_add_u32 v169, s44, %[xa1]\n"
      "v_add_u32 v170, s44, %[xa2]\n"
      "v_add_u32 v171, s44, %[xa3]\n"
      "v_mfma_f32_32x32x16_bf16 v[112:127], v[164:167], v[156:159], v[112:127]\n"
      "ds_read_b64_tr_b16 v[148:149], %[wa1] offset:0\n"
      "ds_read_b64_tr_b16 v[150:151], %[wa1] offset:1024\n"
      "ds_read_b128 v[140:143], v172\n"
      "v_mfma_f32_32x32x16_bf16 v[96:111], v[160:163], v[156:159], v[96:111]\n"
      "s_add_u32 m0, m0, 0x400\n"
      "s_add_u32 s46, s46, 15360\n"
      "s_addc_u32 s47, s47, 0\n"
      "global_load_lds_dwordx4 %[wv], s[46:47]\n"
      "s_add_u32 s40, s40, 122880\n"
      "s_cmp_eq_u32 s40, 2457600\n"
      "s_cselect_b32 s40, 0, s40\n"
      "v_xor_b32 v173, 0x1000, v169\n"
      "v_xor_b32 v174, 0x1000, v170\n"
      "v_xor_b32 v175, 0x1000, v171\n"
      ".p2align 3\n"
      "LOOP_%=:\n"
      "s_waitcnt lgkmcnt(3)\n"
      "v_mfma_f32_32x32x16_bf16 v[64:79], v[144:147], v[136:139], v[64:79]\n"
      "ds_read_b128 v[152:155], v169\n"
      "ds_read_b64_tr_b16 v[160:161], %[wa0] offset:4096\n"
      "ds_read_b64_tr_b16 v[162:163], %[wa0] offset:5120\n"
      "s_waitcnt lgkmcnt(4)\n"
      "v_mfma_f32_32x32x16_bf16 v[80:95], v[148:151], v[136:139], v[80:95]\n"
      "s_add_u32 m0, s45, 0\n"
      "s_add_u32 s43, s42, 0\n"
      "s_add_u32 s46, %[xb0], s43\n"
      "s_addc_u32 s47, %[xb1], 0\n"
      "global_load_lds_dwordx4 %[xe], s[46:47]\n"
      "s_waitcnt lgkmcnt(3)\n"
      "v_mfma_f32_32x32x16_bf16 v[112:127], v[148:151], v[140:143], v[112:127]\n"
      "ds_read_b64_tr_b16 v[164:165], %[wa1] offset:4096\n"
      "ds_read_b64_tr_b16 v[166:167], %[wa1] offset:5120\n"
      "ds_read_b128 v[156:159], v173\n"
      "v_mfma_f32_32x32x16_bf16 v[96:111], v[144:147], v[140:143], v[96:111]\n"
      "s_add_u32 m0, m0, 0x400\n"
      "s_add_u32 s46, s46, 10240\n"
      "s_addc_u32 s47, s47, 0\n"
      "global_load_lds_dwordx4 %[xo], s[46:47]\n"
      "v_mfma_f32_16x16x32_bf16 v[128:131], %[sel], v[144:147], v[128:131]\n"
      "s_waitcnt vmcnt(8) lgkmcnt(0)\n"
      "s_barrier\n"
      "v_mfma_f32_16x16x32_bf16 v[128:131], %[sel], v[160:163], v[128:131]\n"
      "v_mfma_f32_32x32x16_bf16 v[64:79], v[160:163], v[152:155], v[64:79]\n"
      "ds_read_b128 v[136:139], v170\n"
      "ds_read_b64_tr_b16 v[144:145], %[wa0] offset:8192\n"
      "ds_read_b64_tr_b16 v[146:147], %[wa0] offset:9216\n"
      "v_mfma_f32_32x32x16_bf16 v[80:95], v[164:167], v[152:155], v[80:95]\n"
      "s_add_u32 m0, %[ldw], 0\n"
      "s_add_u32 s46, %[wb0], s40\n"
      "s_addc_u32 s47, %[wb1], 0\n"
      "global_load_lds_dwordx4 %[wv], s[46:47]\n"
      "v_mfma_f32_32x32x16_bf16 v[112:127], v[164:167], v[156:159], v[112:127]\n"
      "ds_read_b64_tr_b16 v[148:149], %[wa1] offset:8192\n"
      "ds_read_b64_tr_b16 v[150:151], %[wa1] offset:9216\n"
      "ds_read_b128 v[140:143], v174\n"
      "v_mfma_f32_32x32x16_bf16 v[96:111], v[160:163], v[156:159], v[96:111]\n"
      "s_add_u32 m0, m0, 0x400\n"
      "s_add_u32 s46, s46, 15360\n"
      "s_addc_u32 s47, s47, 0\n"
      "global_load_lds_dwordx4 %[wv], s[46:47]\n"
      "s_add_u32 s40, s40, 122880\n"
      "s_cmp_eq_u32 s40, 2457600\n"
      "s_cselect_b32 s40, 0, s40\n"
      "s_waitcnt lgkmcnt(3)\n"
      "v_mfma_f32_32x32x16_bf16 v[64:79], v[144:147], v[136:139], v[64:79]\n"
      "ds_read_b128 v[152:155], v171\n"
      "ds_read_b64_tr_b16 v[160:161], %[wa0] offset:12288\n"
      "ds_read_b64_tr_b16 v[162:163], %[wa0] offset:13312\n"
      "s_waitcnt lgkmcnt(4)\n"
      "v_mfma_f32_32x32x16_bf16 v[80:95], v[148:151], v[136:139], v[80:95]\n"
      "s_add_u32 m0, s45, 2048\n"
      "s_add_u32 s43, s42, 20480\n"
      "s_add_u32 s46, %[xb0], s43\n"
      "s_addc_u32 s47, %[xb1], 0\n"
      "global_load_lds_dwordx4 %[xe], s[46:47]\n"
      "s_waitcnt lgkmcnt(3)\n"
      "v_mfma_f32_32x32x16_bf16 v[112:127], v[148:151], v[140:143], v[112:127]\n"
      "ds_read_b64_tr_b16 v[164:165], %[wa1] offset:12288\n"
      "ds_read_b64_tr_b16 v[166:167], %[wa1] offset:13312\n"
      "ds_read_b128 v[156:159], v175\n"
      "v_mfma_f32_32x32x16_bf16 v[96:111], v[144:147], v[140:143], v[96:111]\n"
      "s_add_u32 m0, m0, 0x400\n"
      "s_add_u32 s46, s46, 10240\n"
      "s_addc_u32 s47, s47, 0\n"
      "global_load_lds_dwordx4 %[xo], s[46:47]\n"
      "s_add_u32 s42, s42, 128\n"
      "s_cmp_eq_u32 s42, 1280\n"
      "s_cselect_b32 s42, 0, s42\n"
      "s_add_u32 s45, s45, 16384\n"
      "s_cmp_eq_u32 s45, %[ldxe]\n"
      "s_cselect_b32 s45, %[ldx], s45\n"
      "v_mfma_f32_16x16x32_bf16 v[128:131], %[sel], v[144:147], v[128:131]\n"
      "s_add_u32 s44, s44, 16384\n"
      "s_cmp_eq_u32 s44, 49152\n"
      "s_cselect_b32 s44, 0, s44\n"
      "s_waitcnt vmcnt(8) lgkmcnt(0)\n"
      "s_barrier\n"
      "v_mfma_f32_16x16x32_bf16 v[128:131], %[sel], v[160:163], v[128:131]\n"
      "v_mfma_f32_32x32x16_bf16 v[64:79], v[160:163], v[152:155], v[64:79]\n"
      "v_add_u32 v168, s44, %[xa0]\n"
      "v_xor_b32 v172, 0x1000, v168\n"
      "ds_read_b128 v[136:139], v168\n"
      "ds_read_b64_tr_b16 v[144:145], %[wa0] offset:16384\n"
      "ds_read_b64_tr_b16 v[146:147], %[wa0] offset:17408\n"
      "v_mfma_f32_32x32x16_bf16 v[80:95], v[164:167], v[152:155], v[80:95]\n"
      "s_add_u32 m0, %[ldw], 8192\n"
      "s_add_u32 s46, %[wb0], s40\n"
      "s_addc_u32 s47, %[wb1], 0\n"
      "global_load_lds_dwordx4 %[wv], s[46:47]\n"
      "v_add_u32 v169, s44, %[xa1]\n"
      "v_add_u32 v170, s44, %[xa2]\n"
      "v_add_u32 v171, s44, %[xa3]\n"
      "v_mfma_f32_32x32x16_bf16 v[112:127], v[164:167], v[156:159], v[112:127]\n"
      "ds_read_b64_tr_b16 v[148:149], %[wa1] offset:16384\n"
      "ds_read_b64_tr_b16 v[150:151], %[wa1] offset:17408\n"
      "ds_read_b128 v[140:143], v172\n"
      "v_mfma_f32_32x32x16_bf16 v[96:111], v[160:163], v[156:159], v[96:111]\n"
      "s_add_u32 m0, m0, 0x400\n"
      "s_add_u32 s46, s46, 15360\n"
      "s_addc_u32 s47, s47, 0\n"
      "global_load_lds_dwordx4 %[wv], s[46:47]\n"
      "s_add_u32 s40, s40, 122880\n"
      "s_cmp_eq_u32 s40, 2457600\n"
      "s_cselect_b32 s40, 0, s40\n"
      "v_xor_b32 v173, 0x1000, v169\n"
      "v_xor_b32 v174, 0x1000, v170\n"
      "v_xor_b32 v175, 0x1000, v171\n"
      "s_waitcnt lgkmcnt(3)\n"
      "v_mfma_f32_32x32x16_bf16 v[64:79], v[144:147], v[136:139], v[64:79]\n"
      "ds_read_b128 v[152:155], v169\n"
      "ds_read_b64_tr_b16 v[160:161], %[wa0] offset:20480\n"
      "ds_read_b64_tr_b16 v[162:163], %[wa0] offset:21504\n"
      "s_waitcnt lgkmcnt(4)\n"
      "v_mfma_f32_32x32x16_bf16 v[80:95], v[148:151], v[136:139], v[80:95]\n"
      "s_add_u32 m0, s45, 0\n"
      "s_add_u32 s43, s42, 0\n"
      "s_add_u32 s46, %[xb0], s43\n"
      "s_addc_u32 s47, %[xb1], 0\n"
      "global_load_lds_dwordx4 %[xe], s[46:47]\n"
      "s_waitcnt lgkmcnt(3)\n"
      "v_mfma_f32_32x32x16_bf16 v[112:127], v[148:151], v[140:143], v[112:127]\n"
      "ds_read_b64_tr_b16 v[164:165], %[wa1] offset:20480\n"
      "ds_read_b64_tr_b16 v[166:167], %[wa1] offset:21504\n"
      "ds_read_b128 v[156:159], v173\n"
      "v_mfma_f32_32x32x16_bf16 v[96:111], v[144:147], v[140:143], v[96:111]\n"
      "s_add_u32 m0, m0, 0x400\n"
      "s_add_u32 s46, s46, 10240\n"
      "s_addc_u32 s47, s47, 0\n"
      "global_load_lds_dwordx4 %[xo], s[46:47]\n"
      "v_mfma_f32_16x16x32_bf16 v[128:131], %[sel], v[144:147], v[128:131]\n"
      "s_waitcnt vmcnt(8) lgkmcnt(0)\n"
      "s_barrier\n"
      "v_mfma_f32_16x16x32_bf16 v[128:131], %[sel], v[160:163], v[128:131]\n"
      "v_mfma_f32_32x32x16_bf16 v[64:79], v[160:163], v[152:155], v[64:79]\n"
      "ds_read_b128 v[136:139], v170\n"
      "ds_read_b64_tr_b16 v[144:145], %[wa0] offset:24576\n"
      "ds_read_b64_tr_b16 v[146:147], %[wa0] offset:25600\n"
      "v_mfma_f32_32x32x16_bf16 v[80:95], v[164:167], v[152:155], v[80:95]\n"
      "s_add_u32 m0, %[ldw], 16384\n"
      "s_add_u32 s46, %[wb0], s40\n"
      "s_addc_u32 s47, %[wb1], 0\n"
      "global_load_lds_dwordx4 %[wv], s[46:47]\n"
      "v_mfma_f32_32x32x16_bf16 v[112:127], v[164:167], v[156:159], v[112:127]\n"
      "ds_read_b64_tr_b16 v[148:149], %[wa1] offset:24576\n"
      "ds_read_b64_tr_b16 v[150:151], %[wa1] offset:25600\n"
      "ds_read_b128 v[140:143], v174\n"
      "v_mfma_f32_32x32x16_bf16 v[96:111], v[160:163], v[156:159], v[96:111]\n"
      "s_add_u32 m0, m0, 0x400\n"
      "s_add_u32 s46, s46, 15360\n"
      "s_addc_u32 s47, s47, 0\n"
      "global_load_lds_dwordx4 %[wv], s[46:47]\n"
      "s_add_u32 s40, s40, 122880\n"
      "s_cmp_eq_u32 s40, 2457600\n"
      "s_cselect_b32 s40, 0, s40\n"
      "s_waitcnt lgkmcnt(3)\n"
      "v_mfma_f32_32x32x16_bf16 v[64:79], v[144:147], v[136:139], v[64:79]\n"
      "ds_read_b128 v[152:155], v171\n"
      "ds_read_b64_tr_b16 v[160:161], %[wa0] offset:28672\n"
      "ds_read_b64_tr_b16 v[162:163], %[wa0] offset:29696\n"
      "s_waitcnt lgkmcnt(4)\n"
      "v_mfma_f32_32x32x16_bf16 v[80:95], v[148:151], v[136:139], v[80:95]\n"
      "s_add_u32 m0, s45, 2048\n"
      "s_add_u32 s43, s42, 20480\n"
      "s_add_u32 s46, %[xb0], s43\n"
      "s_addc_u32 s47, %[xb1], 0\n"
      "global_load_lds_dwordx4 %[xe], s[46:47]\n"
      "s_waitcnt lgkmcnt(3)\n"
      "v_mfma_f32_32x32x16_bf16 v[112:127], v[148:151], v[140:143], v[112:127]\n"
      "ds_read_b64_tr_b16 v[164:165], %[wa1] offset:28672\n"
      "ds_read_b64_tr_b16 v[166:167], %[wa1] offset:29696\n"
      "ds_read_b128 v[156:159], v175\n"
      "v_mfma_f32_32x32x16_bf16 v[96:111], v[144:147], v[140:143], v[96:111]\n"
      "s_add_u32 m0, m0, 0x400\n"
      "s_add_u32 s46, s46, 10240\n"
      "s_addc_u32 s47, s47, 0\n"
      "global_load_lds_dwordx4 %[xo], s[46:47]\n"
      "s_add_u32 s42, s42, 128\n"
      "s_cmp_eq_u32 s42, 1280\n"
      "s_cselect_b32 s42, 0, s42\n"
      "s_add_u32 s45, s45, 16384\n"
      "s_cmp_eq_u32 s45, %[ldxe]\n"
      "s_cselect_b32 s45, %[ldx], s45\n"
      "v_mfma_f32_16x16x32_bf16 v[128:131], %[sel], v[144:147], v[128:131]\n"
      "s_add_u32 s44, s44, 16384\n"
      "s_cmp_eq_u32 s44, 49152\n"
      "s_cselect_b32 s44, 0, s44\n"
      "s_waitcnt vmcnt(8) lgkmcnt(0)\n"
      "s_barrier\n"
      "v_mfma_f32_16x16x32_bf16 v[128:131], %[sel], v[160:163], v[128:131]\n"
      "v_mfma_f32_32x32x16_bf16 v[64:79], v[160:163], v[152:155], v[64:79]\n"
      "v_add_u32 v168, s44, %[xa0]\n"
      "v_xor_b32 v172, 0x1000, v168\n"
      "ds_read_b128 v[136:139], v168\n"
      "ds_read_b64_tr_b16 v[144:145], %[wa0] offset:0\n"
      "ds_read_b64_tr_b16 v[146:147], %[wa0] offset:1024\n"
      "v_mfma_f32_32x32x16_bf16 v[80:95], v[164:167], v[152:155], v[80:95]\n"
      "s_add_u32 m0, %[ldw], 24576\n"
      "s_add_u32 s46, %[wb0], s40\n"
      "s_addc_u32 s47, %[wb1], 0\n"
      "global_load_lds_dwordx4 %[wv], s[46:47]\n"
      "v_add_u32 v169, s44, %[xa1]\n"
      "v_add_u32 v170, s44, %[xa2]\n"
      "v_add_u32 v171, s44, %[xa3]\n"
      "v_mfma_f32_32x32x16_bf16 v[112:127], v[164:167], v[156:159], v[112:127]\n"
      "ds_read_b64_tr_b16 v[148:149], %[wa1] offset:0\n"
      "ds_read_b64_tr_b16 v[150:151], %[wa1] offset:1024\n"
      "ds_read_b128 v[140:143], v172\n"
      "v_mfma_f32_32x32x16_bf16 v[96:111], v[160:163], v[156:159], v[96:111]\n"
      "s_add_u32 m0, m0, 0x400\n"
      "s_add_u32 s46, s46, 15360\n"
      "s_addc_u32 s47, s47, 0\n"
      "global_load_lds_dwordx4 %[wv], s[46:47]\n"
      "s_add_u32 s40, s40, 122880\n"
      "s_cmp_eq_u32 s40, 2457600\n"
      "s_cselect_b32 s40, 0, s40\n"
      "v_xor_b32 v173, 0x1000, v169\n"
      "v_xor_b32 v174, 0x1000, v170\n"
      "v_xor_b32 v175, 0x1000, v171\n"
      "s_sub_u32 s36, s36, 1\n"
      "s_cmp_lg_u32 s36, 0\n"
      "s_cbranch_scc1 LOOP_%=\n"
      "s_waitcnt lgkmcnt(3)\n"
      "v_mfma_f32_32x32x16_bf16 v[64:79], v[144:147], v[136:139], v[64:79]\n"
      "ds_read_b128 v[152:155], v169\n"
      "ds_read_b64_tr_b16 v[160:161], %[wa0] offset:4096\n"
      "ds_read_b64_tr_b16 v[162:163], %[wa0] offset:5120\n"
      "s_waitcnt lgkmcnt(4)\n"
      "v_mfma_f32_32x32x16_bf16 v[80:95], v[148:151], v[136:139], v[80:95]\n"
      "s_waitcnt lgkmcnt(3)\n"
      "v_mfma_f32_32x32x16_bf16 v[112:127], v[148:151], v[140:143], v[112:127]\n"
      "ds_read_b64_tr_b16 v[164:165], %[wa1] offset:4096\n"
      "ds_read_b64_tr_b16 v[166:167], %[wa1] offset:5120\n"
      "ds_read_b128 v[156:159], v173\n"
      "v_mfma_f32_32x32x16_bf16 v[96:111], v[144:147], v[140:143], v[96:111]\n"
      "v_mfma_f32_16x16x32_bf16 v[128:131], %[sel], v[144:147], v[128:131]\n"
      "s_waitcnt vmcnt(6) lgkmcnt(0)\n"
      "s_barrier\n"
      "v_mfma_f32_16x16x32_bf16 v[128:131], %[sel], v[160:163], v[128:131]\n"
      "v_mfma_f32_32x32x16_bf16 v[64:79], v[160:163], v[152:155], v[64:79]\n"
      "ds_read_b128 v[136:139], v170\n"
      "ds_read_b64_tr_b16 v[144:145], %[wa0] offset:8192\n"
      "ds_read_b64_tr_b16 v[146:147], %[wa0] offset:9216\n"
      "v_mfma_f32_32x32x16_bf16 v[80:95], v[164:167], v[152:155], v[80:95]\n"
      "global_load_dwordx4 v[132:135], %[gv], %[gb] sc1\n"
      "v_mfma_f32_32x32x16_bf16 v[112:127], v[164:167], v[156:159], v[112:127]\n"
      "ds_read_b64_tr_b16 v[148:149], %[wa1] offset:8192\n"
      "ds_read_b64_tr_b16 v[150:151], %[wa1] offset:9216\n"
      "ds_read_b128 v[140:143], v174\n"
      "v_mfma_f32_32x32x16_bf16 v[96:111], v[160:163], v[156:159], v[96:111]\n"
      "v_mov_b32 v176, 0\n"
      "global_load_dwordx2 v[176:177], v176, %[ep] sc1\n"
      "s_waitcnt lgkmcnt(3)\n"
      "v_mfma_f32_32x32x16_bf16 v[64:79], v[144:147], v[136:139], v[64:79]\n"
      "ds_read_b128 v[152:155], v171\n"
      "ds_read_b64_tr_b16 v[160:161], %[wa0] offset:12288\n"
      "ds_read_b64_tr_b16 v[162:163], %[wa0] offset:13312\n"
      "s_waitcnt lgkmcnt(4)\n"
      "v_mfma_f32_32x32x16_bf16 v[80:95], v[148:151], v[136:139], v[80:95]\n"
      "s_waitcnt lgkmcnt(3)\n"
      "v_mfma_f32_32x32x16_bf16 v[112:127], v[148:151], v[140:143], v[112:127]\n"
      "ds_read_b64_tr_b16 v[164:165], %[wa1] offset:12288\n"
      "ds_read_b64_tr_b16 v[166:167], %[wa1] offset:13312\n"
      "ds_read_b128 v[156:159], v175\n"
      "v_mfma_f32_32x32x16_bf16 v[96:111], v[144:147], v[140:143], v[96:111]\n"
      "v_mfma_f32_16x16x32_bf16 v[128:131], %[sel], v[144:147], v[128:131]\n"
      "s_add_u32 s44, s44, 16384\n"
      "s_cmp_eq_u32 s44, 49152\n"
      "s_cselect_b32 s44, 0, s44\n"
      "s_waitcnt vmcnt(4) lgkmcnt(0)\n"
      "s_barrier\n"
      "v_mfma_f32_16x16x32_bf16 v[128:131], %[sel], v[160:163], v[128:131]\n"
      "v_mfma_f32_32x32x16_bf16 v[64:79], v[160:163], v[152:155], v[64:79]\n"
      "v_add_u32 v168, s44, %[xa0]\n"
      "v_xor_b32 v172, 0x1000, v168\n"
      "ds_read_b128 v[136:139], v168\n"
      "ds_read_b64_tr_b16 v[144:145], %[wa0] offset:16384\n"
      "ds_read_b64_tr_b16 v[146:147], %[wa0] offset:17408\n"
      "v_mfma_f32_32x32x16_bf16 v[80:95], v[164:167], v[152:155], v[80:95]\n"
      "v_add_u32 v169, s44, %[xa1]\n"
      "v_add_u32 v170, s44, %[xa2]\n"
      "v_add_u32 v171, s44, %[xa3]\n"
      "v_mfma_f32_32x32x16_bf16 v[112:127], v[164:167], v[156:159], v[112:127]\n"
      "ds_read_b64_tr_b16 v[148:149], %[wa1] offset:16384\n"
      "ds_read_b64_tr_b16 v[150:151], %[wa1] offset:17408\n"
      "ds_read_b128 v[140:143], v172\n"
      "v_mfma_f32_32x32x16_bf16 v[96:111], v[160:163], v[156:159], v[96:111]\n"
      "v_xor_b32 v173, 0x1000, v169\n"
      "v_xor_b32 v174, 0x1000, v170\n"
      "v_xor_b32 v175, 0x1000, v171\n"
      "s_waitcnt lgkmcnt(3)\n"
      "v_mfma_f32_32x32x16_bf16 v[64:79], v[144:147], v[136:139], v[64:79]\n"
      "ds_read_b128 v[152:155], v169\n"
      "ds_read_b64_tr_b16 v[160:161], %[wa0] offset:20480\n"
      "ds_read_b64_tr_b16 v[162:163], %[wa0] offset:21504\n"
      "s_waitcnt lgkmcnt(4)\n"
      "v_mfma_f32_32x32x16_bf16 v[80:95], v[148:151], v[136:139], v[80:95]\n"
      "s_waitcnt lgkmcnt(3)\n"
      "v_mfma_f32_32x32x16_bf16 v[112:127], v[148:151], v[140:143], v[112:127]\n"
      "ds_read_b64_tr_b16 v[164:165], %[wa1] offset:20480\n"
      "ds_read_b64_tr_b16 v[166:167], %[wa1] offset:21504\n"
      "ds_read_b128 v[156:159], v173\n"
      "v_mfma_f32_32x32x16_bf16 v[96:111], v[144:147], v[140:143], v[96:111]\n"
      "v_mfma_f32_16x16x32_bf16 v[128:131], %[sel], v[144:147], v[128:131]\n"
      "s_waitcnt vmcnt(0) lgkmcnt(0)\n"
      "s_barrier\n"
      "v_mfma_f32_16x16x32_bf16 v[128:131], %[sel], v[160:163], v[128:131]\n"
      "v_mfma_f32_32x32x16_bf16 v[64:79], v[160:163], v[152:155], v[64:79]\n"
      "ds_read_b128 v[136:139], v170\n"
      "ds_read_b64_tr_b16 v[144:145], %[wa0] offset:24576\n"
      "ds_read_b64_tr_b16 v[146:147], %[wa0] offset:25600\n"
      "v_mfma_f32_32x32x16_bf16 v[80:95], v[164:167], v[152:155], v[80:95]\n"
      "v_mfma_f32_32x32x16_bf16 v[112:127], v[164:167], v[156:159], v[112:127]\n"
      "ds_read_b64_tr_b16 v[148:149], %[wa1] offset:24576\n"
      "ds_read_b64_tr_b16 v[150:151], %[wa1] offset:25600\n"
      "ds_read_b128 v[140:143], v174\n"
      "v_mfma_f32_32x32x16_bf16 v[96:111], v[160:163], v[156:159], v[96:111]\n"
      "s_waitcnt lgkmcnt(3)\n"
      "v_mfma_f32_32x32x16_bf16 v[64:79], v[144:147], v[136:139], v[64:79]\n"
      "ds_read_b128 v[152:155], v171\n"
      "ds_read_b64_tr_b16 v[160:161], %[wa0] offset:28672\n"
      "ds_read_b64_tr_b16 v[162:163], %[wa0] offset:29696\n"
      "v_readfirstlane_b32 %[tag0], v176\n"
      "v_readfirstlane_b32 %[tag1], v177\n"
      "s_waitcnt lgkmcnt(4)\n"
      "v_mfma_f32_32x32x16_bf16 v[80:95], v[148:151], v[136:139], v[80:95]\n"
      "ds_read_b64_tr_b16 v[164:165], %[wa1] offset:28672\n"
      "ds_read_b64_tr_b16 v[166:167], %[wa1] offset:29696\n"
      "ds_read_b128 v[156:159], v175\n"
      "s_add_u32 %[tag0], %[tag0], 1\n"
      "s_addc_u32 %[tag1], %[tag1], 0\n"
      "s_waitcnt lgkmcnt(6)\n"
      "v_mfma_f32_32x32x16_bf16 v[112:127], v[148:151], v[140:143], v[112:127]\n"
      "v_lshlrev_b32 v168, 16, v133\n"
      "v_rcp_f32 v168, v168\n"
      "v_and_b32 v169, 0xffff0000, v135\n"
      "v_xor_b32 v169, 0x80000000, v169\n"
      "v_mul_f32 v168, %[qs], v168\n"
      "v_mfma_f32_32x32x16_bf16 v[96:111], v[144:147], v[140:143], v[96:111]\n"
      "ds_write_b64 %[sw], v[168:169]\n"
      "v_cmp_ne_u32 vcc, %[tag0], v132\n"
      "v_cmp_ne_u32 s[46:47], %[tag1], v134\n"
      "v_mfma_f32_16x16x32_bf16 v[128:131], %[sel], v[144:147], v[128:131]\n"
      "s_waitcnt lgkmcnt(4)\n"
      "v_mfma_f32_16x16x32_bf16 v[128:131], %[sel], v[160:163], v[128:131]\n"
      "v_mfma_f32_32x32x16_bf16 v[64:79], v[160:163], v[152:155], v[64:79]\n"
      "s_or_b64 s[46:47], s[46:47], vcc\n"
      "s_mov_b32 vcc_lo, 0xffff0000\n"
      "s_mov_b32 vcc_hi, 0xffff0000\n"
      "s_waitcnt lgkmcnt(2)\n"
      "v_mfma_f32_32x32x16_bf16 v[80:95], v[164:167], v[152:155], v[80:95]\n"
      "s_nop 2\n"
      "v_cndmask_b32 v170, v128, v129, vcc\n"
      "ds_write_b32 %[cw], v170\n"
      "s_waitcnt lgkmcnt(1)\n"
      "v_mfma_f32_32x32x16_bf16 v[112:127], v[164:167], v[156:159], v[112:127]\n"
      "v_mfma_f32_32x32x16_bf16 v[96:111], v[160:163], v[156:159], v[96:111]\n"
      "s_cmp_eq_u64 s[46:47], 0\n"
      "s_cbranch_scc1 LOK_%=\n"
      "s_mov_b32 s36, 0x10000\n"
      "LBAD_%=:\n"
      "s_sleep 4\n"
      "global_load_dwordx4 v[132:135], %[gv], %[gb] sc1\n"
      "s_waitcnt vmcnt(0)\n"
      "v_cmp_ne_u32 vcc, %[tag0], v132\n"
      "v_cmp_ne_u32 s[46:47], %[tag1], v134\n"
      "s_sub_u32 s36, s36, 1\n"
      "s_cbranch_scc1 LGIVE_%=\n"
      "s_or_b64 s[46:47], s[46:47], vcc\n"
      "s_cbranch_scc1 LBAD_%=\n"
      "LGIVE_%=:\n"
      "v_lshlrev_b32 v168, 16, v133\n"
      "v_rcp_f32 v168, v168\n"
      "v_and_b32 v169, 0xffff0000, v135\n"
      "v_xor_b32 v169, 0x80000000, v169\n"
      "v_mul_f32 v168, %[qs], v168\n"
      "ds_write_b64 %[sw], v[168:169]\n"
      "LOK_%=:\n"
      "s_waitcnt lgkmcnt(0)\n"
      "s_barrier\n"
      "s_nop 7\n"
      QKV_OPS : "v128", "v129", "v130", "v131", "v132", "v133", "v134", "v135", "v136", "v137", "v138", "v139", "v140", "v141", "v142", "v143", "v144", "v145", "v146", "v147", "v148", "v149", "v150", "v151", "v152", "v153", "v154", "v155", "v156", "v157", "v158", "v159", "v160", "v161", "v162", "v163", "v164", "v165", "v166", "v167", "v168", "v169", "v170", "v171", "v172", "v173", "v174", "v175", "v176", "v177", "s36", "s40", "s41", "s42", "s43", "s44", "s45", "s46", "s47", "scc", "memory", "vcc"
    );
  }
  STAMP(ws, 0, 2);
  if (QDELAY && !isq && ((tok0 >> 7) & 6) != 0) __builtin_amdgcn_s_sleep(QDELAY);
  const LAS char* stat = sm + 16384; const LAS char* csum = sm + 16384 + 1024;
  if (VT) {
    float c[2];
#pragma unroll
    for (int j = 0; j < 2; ++j) c[j] = *(const LAS float*)(csum + (la[1] ^ (j * 128)));
#pragma unroll
    for (int i = 0; i < 2; ++i)
#pragma unroll
      for (int s = 0; s < 2; ++s) {
        f32x4 sv4[4];
#pragma unroll
        for (int e = 0; e < 4; ++e) sv4[e] = *(const LAS f32x4*)(stat + (la[0] ^ (i * 256)) + (16 * s + 8 * (e >> 1) + 2 * (e & 1)) * 8);
#pragma unroll
        for (int j = 0; j < 2; ++j) {
          u32x4 o;
#pragma unroll
          for (int e = 0; e < 4; ++e)
            o[e] = pk_bf16(__builtin_fmaf(sv4[e][1], c[j], acc[i][j][8 * s + 2 * e]) * sv4[e][0], __builtin_fmaf(sv4[e][3], c[j], acc[i][j][8 * s + 2 * e + 1]) * sv4[e][2]);
          st128(rs, so[j][0] + s * 672, base[i], o);
        }
      }
  } else {
    f32x2 st[2]; f32x4 c[2][4];
#pragma unroll
    for (int i = 0; i < 2; ++i) st[i] = *(const LAS f32x2*)(stat + (la[0] ^ (i * 256)));
#pragma unroll
    for (int j = 0; j < 2; ++j)
#pragma unroll
      for (int g = 0; g < 4; ++g) c[j][g] = *(const LAS f32x4*)(csum + (la[1] ^ (j * 128)) + 32 * g);
#pragma unroll
    for (int j = 0; j < 2; ++j)
#pragma unroll
      for (int g = 0; g < 4; ++g)
#pragma unroll
        for (int i = 0; i < 2; ++i)
          st64(rs, so[j][g], base[i], u32x2{pk_bf16(__builtin_fmaf(st[i][1], c[j][g][0], acc[i][j][4 * g]) * st[i][0], __builtin_fmaf(st[i][1], c[j][g][1], acc[i][j][4 * g + 1]) * st[i][0]),
                                            pk_bf16(__builtin_fmaf(st[i][1], c[j][g][2], acc[i][j][4 * g + 2]) * st[i][0], __builtin_fmaf(st[i][1], c[j][g][3], acc[i][j][4 * g + 3]) * st[i][0])});
  }
  return ((u64)tag1 << 32) | tag0;
}

DI bf16x8 cat2(u32x2 a, u32x2 b) { return __builtin_bit_cast(bf16x8, u32x4{a[0], a[1], b[0], b[1]}); }
DI float max16(const f32x16& s) {
  float a = __builtin_fmaxf(s[0], s[1]);
#pragma unroll
  for (int i = 2; i < 16; i += 2) a = __builtin_fmaxf(__builtin_fmaxf(a, s[i]), s[i + 1]);
  return swap32_max(a);
}
DI void load_q(rsrc_t rs, unsigned qo, int r, int h, bf16x8& qm, u32x2& qt) {
  qm = cat2(ld64(rs, h * 512 + r * 8, qo), ld64(rs, h * 512 + r * 8 + 256, qo));
  const u32x2 t = ld64(rs, r * 8 + 1024, qo);
  qt = h ? u32x2{0u, 0u} : t;
}
DI void attn_slow(f32x16& o, rsrc_t rs, unsigned qo, unsigned kv, int lane) {
  const int r = lane & 31, h = lane >> 5;
  bf16x8 qm; u32x2 qt;
  load_q(rs, qo, r, h, qm, qt);
  const bf16x8 qtail = cat2(qt, u32x2{0u, 0u});
  const unsigned vp = 1280 + (r < 20 ? h * 22 + r : (r == 20 ? 20 : 21)) * 16;
  float m = -__builtin_inff();
  f32x16 acc = {};
#pragma unroll 1
  for (int t = 0; t < 64; ++t) {
    const unsigned ko = kv + kv_tile_off(t), vo = ko;
    const bf16x8 km = cat2(ld64(rs, h * 512 + r * 8, ko), ld64(rs, h * 512 + r * 8 + 256, ko)), ktl = cat2(ld64(rs, r * 8 + 1024, ko), u32x2{0u, 0u});
    const bf16x8 v0 = __builtin_bit_cast(bf16x8, ld128(rs, vp, vo)), v1 = __builtin_bit_cast(bf16x8, ld128(rs, vp + 672, vo));
    f32x16 s = {};
    s = MFMA32(km, qm, s); s = MFMA32(ktl, qtail, s);
    const float mn = __builtin_fmaxf(m, max16(s));
    const float mref = (mn == -__builtin_inff()) ? 0.f : mn;
    const float alpha = __builtin_amdgcn_exp2f(m - mref);
    m = mn;
    u32x4 p0, p1;
#pragma unroll
    for (int i = 0; i < 4; ++i) {
      p0[i] = pk_bf16(__builtin_amdgcn_exp2f(s[2 * i] - mref), __builtin_amdgcn_exp2f(s[2 * i + 1] - mref));
      p1[i] = pk_bf16(__builtin_amdgcn_exp2f(s[8 + 2 * i] - mref), __builtin_amdgcn_exp2f(s[9 + 2 * i] - mref));
    }
#pragma unroll
    for (int i = 0; i < 16; ++i) acc[i] *= alpha;
    acc = MFMA32(v0, __builtin_bit_cast(bf16x8, p0), acc); acc = MFMA32(v1, __builtin_bit_cast(bf16x8, p1), acc);
  }
  o = acc;
}
DI float row_sum(const f32x16& o) {
  auto rr = __builtin_amdgcn_permlane32_swap(__float_as_uint(o[8]), __float_as_uint(o[8]), false, false);
  return __uint_as_float(rr[1]);
}
DI void attn_store(const f32x16& o, float l, rsrc_t rs, bool odd, bool low_odd, unsigned vab, unsigned vc, unsigned sa, unsigned sb, unsigned sc) {
  const float rl = __builtin_amdgcn_rcpf(l);
  u32x2 v[3];
#pragma unroll
  for (int g = 0; g < 3; ++g) {
    const f32x2 a = f32x2{o[4 * g], o[4 * g + 1]} * rl, b = f32x2{o[4 * g + 2], o[4 * g + 3]} * rl;
    v[g] = u32x2{pk_bf16(a[0], a[1]), pk_bf16(b[0], b[1])};
  }
  st64(rs, vab, sa, low_odd ? v[1] : v[0]);
  st64(rs, vab, sb, low_odd ? v[2] : v[1]);
  st64(rs, vc, sc, odd ? v[0] : v[2]);
}
DI void attn_unit(char* ws, LAS char* sm, int head, int qb, int w, int lane, u64 tag) {
  const int r = lane & 31, h = lane >> 5, qg = w & 1, kh = w >> 1;
  const char* kv = ws + WS_KV + head * KV_HEAD;
  const unsigned l0 = lds_addr(sm), voff = w * 5120 + lane * 16;
  const rsrc_t rs = ws_rsrc(ws);
  const unsigned qo = WS_QI + (head * 64 + qb * 8 + 4 * qg) * 1280;
  const int f = 20 * head + 19 * ((lane >> 4) & 1) + 640 + 640 * h, fq = 20 * head + 19 * ((lane >> 4) & 1);
  const unsigned fo = WS_QKF + ((f >> 7) * 16 + (lane & 15)) * QS;
  const unsigned f0 = WS_QKF + ((lane & 4) ? (fq >> 7) * 16 + 2 * qb + (lane & 1) : (f >> 7) * 16 + (lane & 1) + 4 * (lane & 2)) * QS;
  const unsigned vk0 = l0 + kh * 10752 + h * 512 + r * 8;
  const unsigned vv0 = l0 + kh * 10752 + (r < 20 ? h * 22 + r : (r == 20 ? 20 : 21)) * 16;
  const unsigned vq = l0 + 2 * KV_CHUNK + 4 * qg * 1280 + h * 512 + r * 8;
  const bool odd = head & 1;
  unsigned vab = r * 16 + (odd ? 8 - 8 * h : 8 * h), vc = h ? WS_ALL : r * 16 + (odd ? 8 : 0);
  const unsigned obase = WS_X2 + ((5 * head >> 1) * S_ + qb * 256 + qg * 128 + kh * 64) * 16;
  unsigned sa = __builtin_amdgcn_readfirstlane(obase + (head & 1) * (S_ * 16)), sc = __builtin_amdgcn_readfirstlane(obase + (~head & 1) * (2 * S_ * 16));
  unsigned sb = sa + S_ * 16;
  unsigned op[12] = {vq + (2 - 2 * kh) * 1280, vq + 2 * kh * 1280,
                     vk0, l0 + kh * 10752 + (h ? 576 : r * 8), vv0, l0 + h * 512 + r * 8, l0 + (h ? 576 : r * 8),
                     l0 + 2 * KV_CHUNK + (w == 3 ? 23552 : w * 6144) + lane * 16, l0 + 2 * KV_CHUNK + (w == 1 ? 23552 : (w ^ 2) * 6144) + lane * 16,
                     l0 + 3 * KV_CHUNK + w * 256 + lane * 4, l0 + 3 * KV_CHUNK + (lane & 3) * 260, WS_HF + (qb * 32 + r) * HS};
#pragma unroll
  for (int i = 0; i < 12; ++i) asm volatile("" : "+v"(op[i]));
  asm volatile("" : "+v"(vab), "+v"(vc), "+s"(sa), "+s"(sb), "+s"(sc));
  await(ws, f0, ~0u, tag, w);
  if (GPOS == 1 && w == 1 && lane == 0) st_flag(ws, WS_GF + blockIdx.x * 128, tag);
  if (blockIdx.x >= 240 && w == 2 && lane == 0) st_flag(ws, WS_QKF + blockIdx.x * QS, tag);
  STAMP(ws, 0, 4);
#pragma unroll
  for (int i = 0; i < 5; ++i) glds16c(kv + i * 1024, voff, l0 + w * 5120 + i * 1024);
  if (w == 3) glds16c(kv + 5120, voff, l0 + 20480);
#pragma unroll
  for (int i = 0; i < 3; ++i)
    if (i == 0 || w < 3) glds16c(ws + WS_QI + (head * 64 + qb * 8) * 1280 + (3 * w + i) * 1024, lane * 16, l0 + 2 * KV_CHUNK + (3 * w + i) * 1024);
  *(LAS unsigned*)(sm + 3 * KV_CHUNK + w * 256 + lane * 4) = 0u;
  WAIT_V(0);
#pragma unroll
  for (int i = 0; i < 5; ++i) glds16c(kv + KV_CHUNK + i * 1024, voff, l0 + KV_CHUNK + w * 5120 + i * 1024);
  if (w == 3) glds16c(kv + KV_CHUNK + 5120, voff, l0 + KV_CHUNK + 20480);
  WAIT_L0(); BAR();
  STAMP(ws, 0, 5);
  f32x16 o0, o1;
#define ADBG_OP
  asm volatile(
      "v_mov_b32 v232, 0\n"
      "v_mov_b32 v233, 0\n"
      "v_mov_b32 v234, 0\n"
      "v_mov_b32 v235, 0\n"
      "v_mov_b32 v236, 0\n"
      "v_mov_b32 v237, 0\n"
      "v_mov_b32 v238, 0\n"
      "v_mov_b32 v239, 0\n"
      "ds_read_b64 v[120:121], %[vm0]\n"
      "ds_read_b64 v[122:123], %[vm0] offset:256\n"
      "ds_read_b64 v[216:217], %[vq0] offset:0\n"
      "ds_read_b64 v[218:219], %[vq0] offset:256\n"
      "ds_read_b64 v[220:221], %[vq0] offset:1280\n"
      "ds_read_b64 v[222:223], %[vq0] offset:1536\n"
      "ds_read_b64 v[224:225], %[vq1] offset:0\n"
      "ds_read_b64 v[226:227], %[vq1] offset:256\n"
      "ds_read_b64 v[228:229], %[vq1] offset:1280\n"
      "ds_read_b64 v[230:231], %[vq1] offset:1536\n"
      "ds_read_b64 v[124:125], %[vmt0] offset:1024\n"
      "s_mov_b32 exec_hi, 0\n"
      "ds_read_b64 v[232:233], %[vq0] offset:1024\n"
      "ds_read_b64 v[234:235], %[vq0] offset:2304\n"
      "ds_read_b64 v[236:237], %[vq1] offset:1024\n"
      "ds_read_b64 v[238:239], %[vq1] offset:2304\n"
      "s_mov_b32 exec_hi, -1\n"
      "s_mov_b64 s[40:41], %[gb]\n"
      "v_add_u32 v144, 0x1000, %[voff]\n"
      "s_mov_b32 vcc_lo, 0\n"
      "s_mov_b32 vcc_hi, -1\n"
      "s_waitcnt lgkmcnt(11)\n"
      "v_mfma_f32_32x32x16_bf16 v[152:167], v[120:123], v[216:219], 0\n"
      "s_waitcnt lgkmcnt(9)\n"
      "v_mfma_f32_32x32x16_bf16 v[168:183], v[120:123], v[220:223], 0\n"
      "s_waitcnt lgkmcnt(7)\n"
      "v_mfma_f32_32x32x16_bf16 v[184:199], v[120:123], v[224:227], 0\n"
      "s_waitcnt lgkmcnt(5)\n"
      "v_mfma_f32_32x32x16_bf16 v[200:215], v[120:123], v[228:231], 0\n"
      "s_waitcnt lgkmcnt(0)\n"
      "ds_read_b64 v[112:113], %[vk0] offset:0\n"
      "ds_read_b64 v[114:115], %[vk0] offset:256\n"
      "ds_read_b64 v[116:117], %[vkt0] offset:1024\n"
      "ds_read_b128 v[128:131], %[vv0] offset:1280\n"
      "ds_read_b128 v[132:135], %[vv0] offset:1952\n"
      "v_mfma_f32_32x32x8_bf16 v[152:167], v[124:125], v[232:233], v[152:167]\n"
      "v_mfma_f32_32x32x8_bf16 v[168:183], v[124:125], v[234:235], v[168:183]\n"
      "v_mfma_f32_32x32x8_bf16 v[184:199], v[124:125], v[236:237], v[184:199]\n"
      "v_mfma_f32_32x32x8_bf16 v[200:215], v[124:125], v[238:239], v[200:215]\n"
      "s_nop 8\n"
      "v_max3_f32 v152, v152, v153, v154\n"
      "v_max3_f32 v152, v152, v155, v156\n"
      "v_max3_f32 v152, v152, v157, v158\n"
      "v_max3_f32 v152, v152, v159, v160\n"
      "v_max3_f32 v152, v152, v161, v162\n"
      "v_max3_f32 v152, v152, v163, v164\n"
      "v_max3_f32 v152, v152, v165, v166\n"
      "v_max_f32 v152, v152, v167\n"
      "v_cvt_pk_bf16_f32 v152, -v152, 0\n"
      "v_cndmask_b32 v232, v232, v152, vcc\n"
      "s_waitcnt lgkmcnt(0)\n"
      "v_mfma_f32_32x32x16_bf16 v[64:79], v[112:115], v[216:219], 0\n"
      "v_max3_f32 v168, v168, v169, v170\n"
      "v_max3_f32 v168, v168, v171, v172\n"
      "v_max3_f32 v168, v168, v173, v174\n"
      "v_max3_f32 v168, v168, v175, v176\n"
      "v_max3_f32 v168, v168, v177, v178\n"
      "v_max3_f32 v168, v168, v179, v180\n"
      "v_max3_f32 v168, v168, v181, v182\n"
      "v_max_f32 v168, v168, v183\n"
      "v_cvt_pk_bf16_f32 v168, -v168, 0\n"
      "v_cndmask_b32 v234, v234, v168, vcc\n"
      "v_mfma_f32_32x32x8_bf16 v[64:79], v[116:117], v[232:233], v[64:79]\n"
      "v_max3_f32 v184, v184, v185, v186\n"
      "v_max3_f32 v184, v184, v187, v188\n"
      "v_max3_f32 v184, v184, v189, v190\n"
      "v_max3_f32 v184, v184, v191, v192\n"
      "v_max3_f32 v184, v184, v193, v194\n"
      "v_max3_f32 v184, v184, v195, v196\n"
      "v_max3_f32 v184, v184, v197, v198\n"
      "v_max_f32 v184, v184, v199\n"
      "v_cvt_pk_bf16_f32 v184, -v184, 0\n"
      "v_cndmask_b32 v236, v236, v184, vcc\n"
      "v_max3_f32 v200, v200, v201, v202\n"
      "v_max3_f32 v200, v200, v203, v204\n"
      "v_max3_f32 v200, v200, v205, v206\n"
      "v_max3_f32 v200, v200, v207, v208\n"
      "v_max3_f32 v200, v200, v209, v210\n"
      "v_max3_f32 v200, v200, v211, v212\n"
      "v_max3_f32 v200, v200, v213, v214\n"
      "v_max_f32 v200, v200, v215\n"
      "v_cvt_pk_bf16_f32 v200, -v200, 0\n"
      "v_cndmask_b32 v238, v238, v200, vcc\n"
      "v_exp_f32 v64, v64\n"
      "v_exp_f32 v65, v65\n"
      "v_exp_f32 v66, v66\n"
      "v_mfma_f32_32x32x16_bf16 v[80:95], v[112:115], v[220:223], 0\n"
      "v_exp_f32 v67, v67\n"
      "v_exp_f32 v68, v68\n"
      "v_exp_f32 v69, v69\n"
      "v_exp_f32 v70, v70\n"
      "v_exp_f32 v71, v71\n"
      "v_exp_f32 v72, v72\n"
      "v_mfma_f32_32x32x8_bf16 v[80:95], v[116:117], v[234:235], v[80:95]\n"
      "v_exp_f32 v73, v73\n"
      "ds_read_b64 v[120:121], %[vk0] offset:2624\n"
      "ds_read_b64 v[122:123], %[vk0] offset:2880\n"
      "ds_read_b64 v[124:125], %[vkt0] offset:3648\n"
      "ds_read_b128 v[136:139], %[vv0] offset:3904\n"
      "ds_read_b128 v[140:143], %[vv0] offset:4576\n"
      "v_cvt_pk_bf16_f32 v96, v64, v65\n"
      "v_exp_f32 v74, v74\n"
      "v_cvt_pk_bf16_f32 v97, v66, v67\n"
      "v_exp_f32 v75, v75\n"
      "v_cvt_pk_bf16_f32 v98, v68, v69\n"
      "v_exp_f32 v76, v76\n"
      "v_cvt_pk_bf16_f32 v99, v70, v71\n"
      "v_exp_f32 v77, v77\n"
      "v_cvt_pk_bf16_f32 v100, v72, v73\n"
      "v_exp_f32 v78, v78\n"
      "v_exp_f32 v79, v79\n"
      "v_cvt_pk_bf16_f32 v101, v74, v75\n"
      "v_cvt_pk_bf16_f32 v102, v76, v77\n"
      "v_cvt_pk_bf16_f32 v103, v78, v79\n"
      "v_mfma_f32_32x32x16_bf16 v[152:167], v[128:131], v[96:99], 0\n"
      "v_exp_f32 v80, v80\n"
      "v_exp_f32 v81, v81\n"
      "v_exp_f32 v82, v82\n"
      "v_mfma_f32_32x32x16_bf16 v[64:79], v[112:115], v[224:227], 0\n"
      "v_exp_f32 v83, v83\n"
      "v_exp_f32 v84, v84\n"
      "v_exp_f32 v85, v85\n"
      "v_mfma_f32_32x32x16_bf16 v[152:167], v[132:135], v[100:103], v[152:167]\n"
      "v_exp_f32 v86, v86\n"
      "v_exp_f32 v87, v87\n"
      "v_exp_f32 v88, v88\n"
      "v_mfma_f32_32x32x8_bf16 v[64:79], v[116:117], v[236:237], v[64:79]\n"
      "v_exp_f32 v89, v89\n"
      "v_cvt_pk_bf16_f32 v104, v80, v81\n"
      "v_exp_f32 v90, v90\n"
      "v_cvt_pk_bf16_f32 v105, v82, v83\n"
      "v_exp_f32 v91, v91\n"
      "v_cvt_pk_bf16_f32 v106, v84, v85\n"
      "v_exp_f32 v92, v92\n"
      "v_cvt_pk_bf16_f32 v107, v86, v87\n"
      "v_exp_f32 v93, v93\n"
      "v_cvt_pk_bf16_f32 v108, v88, v89\n"
      "v_exp_f32 v94, v94\n"
      "v_exp_f32 v95, v95\n"
      "v_cvt_pk_bf16_f32 v109, v90, v91\n"
      "v_cvt_pk_bf16_f32 v110, v92, v93\n"
      "v_cvt_pk_bf16_f32 v111, v94, v95\n"
      "v_mfma_f32_32x32x16_bf16 v[168:183], v[128:131], v[104:107], 0\n"
      "v_exp_f32 v64, v64\n"
      "v_exp_f32 v65, v65\n"
      "v_exp_f32 v66, v66\n"
      "v_mfma_f32_32x32x16_bf16 v[80:95], v[112:115], v[228:231], 0\n"
      "v_exp_f32 v67, v67\n"
      "v_exp_f32 v68, v68\n"
      "v_exp_f32 v69, v69\n"
      "v_mfma_f32_32x32x16_bf16 v[168:183], v[132:135], v[108:111], v[168:183]\n"
      "v_exp_f32 v70, v70\n"
      "v_exp_f32 v71, v71\n"
      "v_exp_f32 v72, v72\n"
      "v_mfma_f32_32x32x8_bf16 v[80:95], v[116:117], v[238:239], v[80:95]\n"
      "v_exp_f32 v73, v73\n"
      "v_cvt_pk_bf16_f32 v96, v64, v65\n"
      "v_exp_f32 v74, v74\n"
      "v_cvt_pk_bf16_f32 v97, v66, v67\n"
      "v_exp_f32 v75, v75\n"
      "v_cvt_pk_bf16_f32 v98, v68, v69\n"
      "v_exp_f32 v76, v76\n"
      "v_cvt_pk_bf16_f32 v99, v70, v71\n"
      "v_exp_f32 v77, v77\n"
      "v_cvt_pk_bf16_f32 v100, v72, v73\n"
      "v_exp_f32 v78, v78\n"
      "v_exp_f32 v79, v79\n"
      "v_cvt_pk_bf16_f32 v101, v74, v75\n"
      "v_cvt_pk_bf16_f32 v102, v76, v77\n"
      "v_cvt_pk_bf16_f32 v103, v78, v79\n"
      "s_waitcnt lgkmcnt(0)\n"
      "v_mfma_f32_32x32x16_bf16 v[184:199], v[128:131], v[96:99], 0\n"
      "v_exp_f32 v80, v80\n"
      "v_exp_f32 v81, v81\n"
      "v_exp_f32 v82, v82\n"
      "v_mfma_f32_32x32x16_bf16 v[64:79], v[120:123], v[216:219], 0\n"
      "v_exp_f32 v83, v83\n"
      "v_exp_f32 v84, v84\n"
      "v_exp_f32 v85, v85\n"
      "v_mfma_f32_32x32x16_bf16 v[184:199], v[132:135], v[100:103], v[184:199]\n"
      "v_exp_f32 v86, v86\n"
      "v_exp_f32 v87, v87\n"
      "v_exp_f32 v88, v88\n"
      "v_mfma_f32_32x32x8_bf16 v[64:79], v[124:125], v[232:233], v[64:79]\n"
      "v_exp_f32 v89, v89\n"
      "v_cvt_pk_bf16_f32 v104, v80, v81\n"
      "v_exp_f32 v90, v90\n"
      "v_cvt_pk_bf16_f32 v105, v82, v83\n"
      "v_exp_f32 v91, v91\n"
      "v_cvt_pk_bf16_f32 v106, v84, v85\n"
      "v_exp_f32 v92, v92\n"
      "v_cvt_pk_bf16_f32 v107, v86, v87\n"
      "v_exp_f32 v93, v93\n"
      "v_cvt_pk_bf16_f32 v108, v88, v89\n"
      "v_exp_f32 v94, v94\n"
      "v_exp_f32 v95, v95\n"
      "v_cvt_pk_bf16_f32 v109, v90, v91\n"
      "v_cvt_pk_bf16_f32 v110, v92, v93\n"
      "v_cvt_pk_bf16_f32 v111, v94, v95\n"
      "v_mfma_f32_32x32x16_bf16 v[200:215], v[128:131], v[104:107], 0\n"
      "v_exp_f32 v64, v64\n"
      "v_exp_f32 v65, v65\n"
      "v_exp_f32 v66, v66\n"
      "v_mfma_f32_32x32x16_bf16 v[80:95], v[120:123], v[220:223], 0\n"
      "v_exp_f32 v67, v67\n"
      "v_exp_f32 v68, v68\n"
      "v_exp_f32 v69, v69\n"
      "v_mfma_f32_32x32x16_bf16 v[200:215], v[132:135], v[108:111], v[200:215]\n"
      "v_exp_f32 v70, v70\n"
      "v_exp_f32 v71, v71\n"
      "v_exp_f32 v72, v72\n"
      "v_mfma_f32_32x32x8_bf16 v[80:95], v[124:125], v[234:235], v[80:95]\n"
      "v_exp_f32 v73, v73\n"
      "ds_read_b64 v[112:113], %[vk0] offset:5248\n"
      "ds_read_b64 v[114:115], %[vk0] offset:5504\n"
      "ds_read_b64 v[116:117], %[vkt0] offset:6272\n"
      "ds_read_b128 v[128:131], %[vv0] offset:6528\n"
      "ds_read_b128 v[132:135], %[vv0] offset:7200\n"
      "v_cvt_pk_bf16_f32 v96, v64, v65\n"
      "v_exp_f32 v74, v74\n"
      "v_cvt_pk_bf16_f32 v97, v66, v67\n"
      "v_exp_f32 v75, v75\n"
      "v_cvt_pk_bf16_f32 v98, v68, v69\n"
      "v_exp_f32 v76, v76\n"
      "v_cvt_pk_bf16_f32 v99, v70, v71\n"
      "v_exp_f32 v77, v77\n"
      "v_cvt_pk_bf16_f32 v100, v72, v73\n"
      "v_exp_f32 v78, v78\n"
      "v_exp_f32 v79, v79\n"
      "v_cvt_pk_bf16_f32 v101, v74, v75\n"
      "v_cvt_pk_bf16_f32 v102, v76, v77\n"
      "v_cvt_pk_bf16_f32 v103, v78, v79\n"
      "v_mfma_f32_32x32x16_bf16 v[152:167], v[136:139], v[96:99], v[152:167]\n"
      "v_exp_f32 v80, v80\n"
      "v_exp_f32 v81, v81\n"
      "v_exp_f32 v82, v82\n"
      "v_mfma_f32_32x32x16_bf16 v[64:79], v[120:123], v[224:227], 0\n"
      "v_exp_f32 v83, v83\n"
      "v_exp_f32 v84, v84\n"
      "v_exp_f32 v85, v85\n"
      "v_mfma_f32_32x32x16_bf16 v[152:167], v[140:143], v[100:103], v[152:167]\n"
      "v_exp_f32 v86, v86\n"
      "v_exp_f32 v87, v87\n"
      "v_exp_f32 v88, v88\n"
      "v_mfma_f32_32x32x8_bf16 v[64:79], v[124:125], v[236:237], v[64:79]\n"
      "v_exp_f32 v89, v89\n"
      "v_cvt_pk_bf16_f32 v104, v80, v81\n"
      "v_exp_f32 v90, v90\n"
      "v_cvt_pk_bf16_f32 v105, v82, v83\n"
      "v_exp_f32 v91, v91\n"
      "v_cvt_pk_bf16_f32 v106, v84, v85\n"
      "v_exp_f32 v92, v92\n"
      "v_cvt_pk_bf16_f32 v107, v86, v87\n"
      "v_exp_f32 v93, v93\n"
      "v_cvt_pk_bf16_f32 v108, v88, v89\n"
      "v_exp_f32 v94, v94\n"
      "v_exp_f32 v95, v95\n"
      "v_cvt_pk_bf16_f32 v109, v90, v91\n"
      "v_cvt_pk_bf16_f32 v110, v92, v93\n"
      "v_cvt_pk_bf16_f32 v111, v94, v95\n"
      "v_mfma_f32_32x32x16_bf16 v[168:183], v[136:139], v[104:107], v[168:183]\n"
      "v_exp_f32 v64, v64\n"
      "v_exp_f32 v65, v65\n"
      "v_exp_f32 v66, v66\n"
      "v_mfma_f32_32x32x16_bf16 v[80:95], v[120:123], v[228:231], 0\n"
      "v_exp_f32 v67, v67\n"
      "v_exp_f32 v68, v68\n"
      "v_exp_f32 v69, v69\n"
      "v_mfma_f32_32x32x16_bf16 v[168:183], v[140:143], v[108:111], v[168:183]\n"
      "v_exp_f32 v70, v70\n"
      "v_exp_f32 v71, v71\n"
      "v_exp_f32 v72, v72\n"
      "v_mfma_f32_32x32x8_bf16 v[80:95], v[124:125], v[238:239], v[80:95]\n"
      "v_exp_f32 v73, v73\n"
      "v_cvt_pk_bf16_f32 v96, v64, v65\n"
      "v_exp_f32 v74, v74\n"
      "v_cvt_pk_bf16_f32 v97, v66, v67\n"
      "v_exp_f32 v75, v75\n"
      "v_cvt_pk_bf16_f32 v98, v68, v69\n"
      "v_exp_f32 v76, v76\n"
      "v_cvt_pk_bf16_f32 v99, v70, v71\n"
      "v_exp_f32 v77, v77\n"
      "global_load_dwordx2 v[148:149], %[f0], %[ws] sc1\n"
      "v_cvt_pk_bf16_f32 v100, v72, v73\n"
      "v_exp_f32 v78, v78\n"
      "v_exp_f32 v79, v79\n"
      "v_cvt_pk_bf16_f32 v101, v74, v75\n"
      "v_cvt_pk_bf16_f32 v102, v76, v77\n"
      "v_cvt_pk_bf16_f32 v103, v78, v79\n"
      "s_waitcnt lgkmcnt(0)\n"
      "v_mfma_f32_32x32x16_bf16 v[184:199], v[136:139], v[96:99], v[184:199]\n"
      "v_exp_f32 v80, v80\n"
      "v_exp_f32 v81, v81\n"
      "v_exp_f32 v82, v82\n"
      "v_mfma_f32_32x32x16_bf16 v[64:79], v[112:115], v[216:219], 0\n"
      "v_exp_f32 v83, v83\n"
      "v_exp_f32 v84, v84\n"
      "v_exp_f32 v85, v85\n"
      "v_mfma_f32_32x32x16_bf16 v[184:199], v[140:143], v[100:103], v[184:199]\n"
      "v_exp_f32 v86, v86\n"
      "v_exp_f32 v87, v87\n"
      "v_exp_f32 v88, v88\n"
      "v_mfma_f32_32x32x8_bf16 v[64:79], v[116:117], v[232:233], v[64:79]\n"
      "v_exp_f32 v89, v89\n"
      "v_cvt_pk_bf16_f32 v104, v80, v81\n"
      "v_exp_f32 v90, v90\n"
      "v_cvt_pk_bf16_f32 v105, v82, v83\n"
      "v_exp_f32 v91, v91\n"
      "v_cvt_pk_bf16_f32 v106, v84, v85\n"
      "v_exp_f32 v92, v92\n"
      "v_cvt_pk_bf16_f32 v107, v86, v87\n"
      "v_exp_f32 v93, v93\n"
      "v_cvt_pk_bf16_f32 v108, v88, v89\n"
      "v_exp_f32 v94, v94\n"
      "v_exp_f32 v95, v95\n"
      "v_cvt_pk_bf16_f32 v109, v90, v91\n"
      "v_cvt_pk_bf16_f32 v110, v92, v93\n"
      "v_cvt_pk_bf16_f32 v111, v94, v95\n"
      "v_mfma_f32_32x32x16_bf16 v[200:215], v[136:139], v[104:107], v[200:215]\n"
      "v_exp_f32 v64, v64\n"
      "v_exp_f32 v65, v65\n"
      "v_exp_f32 v66, v66\n"
      "v_mfma_f32_32x32x16_bf16 v[80:95], v[112:115], v[220:223], 0\n"
      "v_exp_f32 v67, v67\n"
      "v_exp_f32 v68, v68\n"
      "v_exp_f32 v69, v69\n"
      "v_mfma_f32_32x32x16_bf16 v[200:215], v[140:143], v[108:111], v[200:215]\n"
      "v_exp_f32 v70, v70\n"
      "v_exp_f32 v71, v71\n"
      "v_exp_f32 v72, v72\n"
      "v_mfma_f32_32x32x8_bf16 v[80:95], v[116:117], v[234:235], v[80:95]\n"
      "v_exp_f32 v73, v73\n"
      "ds_read_b64 v[120:121], %[vk0] offset:7872\n"
      "ds_read_b64 v[122:123], %[vk0] offset:8128\n"
      "ds_read_b64 v[124:125], %[vkt0] offset:8896\n"
      "ds_read_b128 v[136:139], %[vv0] offset:9152\n"
      "ds_read_b128 v[140:143], %[vv0] offset:9824\n"
      "v_cvt_pk_bf16_f32 v96, v64, v65\n"
      "v_exp_f32 v74, v74\n"
      "v_cvt_pk_bf16_f32 v97, v66, v67\n"
      "v_exp_f32 v75, v75\n"
      "v_cvt_pk_bf16_f32 v98, v68, v69\n"
      "v_exp_f32 v76, v76\n"
      "v_cvt_pk_bf16_f32 v99, v70, v71\n"
      "v_exp_f32 v77, v77\n"
      "v_cvt_pk_bf16_f32 v100, v72, v73\n"
      "v_exp_f32 v78, v78\n"
      "v_exp_f32 v79, v79\n"
      "v_cvt_pk_bf16_f32 v101, v74, v75\n"
      "v_cvt_pk_bf16_f32 v102, v76, v77\n"
      "v_cvt_pk_bf16_f32 v103, v78, v79\n"
      "v_mfma_f32_32x32x16_bf16 v[152:167], v[128:131], v[96:99], v[152:167]\n"
      "v_exp_f32 v80, v80\n"
      "v_exp_f32 v81, v81\n"
      "v_exp_f32 v82, v82\n"
      "v_mfma_f32_32x32x16_bf16 v[64:79], v[112:115], v[224:227], 0\n"
      "v_exp_f32 v83, v83\n"
      "v_exp_f32 v84, v84\n"
      "v_exp_f32 v85, v85\n"
      "v_mfma_f32_32x32x16_bf16 v[152:167], v[132:135], v[100:103], v[152:167]\n"
      "v_exp_f32 v86, v86\n"
      "v_exp_f32 v87, v87\n"
      "v_exp_f32 v88, v88\n"
      "v_mfma_f32_32x32x8_bf16 v[64:79], v[116:117], v[236:237], v[64:79]\n"
      "v_exp_f32 v89, v89\n"
      "v_cvt_pk_bf16_f32 v104, v80, v81\n"
      "v_exp_f32 v90, v90\n"
      "v_cvt_pk_bf16_f32 v105, v82, v83\n"
      "v_exp_f32 v91, v91\n"
      "v_cvt_pk_bf16_f32 v106, v84, v85\n"
      "v_exp_f32 v92, v92\n"
      "v_cvt_pk_bf16_f32 v107, v86, v87\n"
      "v_exp_f32 v93, v93\n"
      "v_cvt_pk_bf16_f32 v108, v88, v89\n"
      "v_exp_f32 v94, v94\n"
      "v_exp_f32 v95, v95\n"
      "v_cvt_pk_bf16_f32 v109, v90, v91\n"
      "v_cvt_pk_bf16_f32 v110, v92, v93\n"
      "v_cvt_pk_bf16_f32 v111, v94, v95\n"
      "v_mfma_f32_32x32x16_bf16 v[168:183], v[128:131], v[104:107], v[168:183]\n"
      "v_exp_f32 v64, v64\n"
      "v_exp_f32 v65, v65\n"
      "v_exp_f32 v66, v66\n"
      "v_mfma_f32_32x32x16_bf16 v[80:95], v[112:115], v[228:231], 0\n"
      "v_exp_f32 v67, v67\n"
      "v_exp_f32 v68, v68\n"
      "v_exp_f32 v69, v69\n"
      "v_mfma_f32_32x32x16_bf16 v[168:183], v[132:135], v[108:111], v[168:183]\n"
      "v_exp_f32 v70, v70\n"
      "v_exp_f32 v71, v71\n"
      "v_exp_f32 v72, v72\n"
      "v_mfma_f32_32x32x8_bf16 v[80:95], v[116:117], v[238:239], v[80:95]\n"
      "v_exp_f32 v73, v73\n"
      "v_mov_b32 v145, 1\n"
      "s_waitcnt vmcnt(0) lgkmcnt(0)\n"
      "ds_write_b32 %[vpost], v145\n"
      "v_cvt_pk_bf16_f32 v96, v64, v65\n"
      "v_exp_f32 v74, v74\n"
      "v_cvt_pk_bf16_f32 v97, v66, v67\n"
      "v_exp_f32 v75, v75\n"
      "v_cvt_pk_bf16_f32 v98, v68, v69\n"
      "v_exp_f32 v76, v76\n"
      "v_cvt_pk_bf16_f32 v99, v70, v71\n"
      "v_exp_f32 v77, v77\n"
      "v_cvt_pk_bf16_f32 v100, v72, v73\n"
      "v_exp_f32 v78, v78\n"
      "v_exp_f32 v79, v79\n"
      "v_cmp_ne_u64 vcc, %[tag], v[148:149]\n"
      "v_cvt_pk_bf16_f32 v101, v74, v75\n"
      "v_cvt_pk_bf16_f32 v102, v76, v77\n"
      "v_cvt_pk_bf16_f32 v103, v78, v79\n"
      "s_cbranch_vccnz LFLAG0_%=\n"
      "LFLAGGO0_%=:\n"
      "v_mfma_f32_32x32x16_bf16 v[184:199], v[128:131], v[96:99], v[184:199]\n"
      "v_exp_f32 v80, v80\n"
      "v_exp_f32 v81, v81\n"
      "v_exp_f32 v82, v82\n"
      "v_mfma_f32_32x32x16_bf16 v[64:79], v[120:123], v[216:219], 0\n"
      "v_exp_f32 v83, v83\n"
      "v_exp_f32 v84, v84\n"
      "v_exp_f32 v85, v85\n"
      "v_mfma_f32_32x32x16_bf16 v[184:199], v[132:135], v[100:103], v[184:199]\n"
      "v_exp_f32 v86, v86\n"
      "v_exp_f32 v87, v87\n"
      "v_exp_f32 v88, v88\n"
      "v_mfma_f32_32x32x8_bf16 v[64:79], v[124:125], v[232:233], v[64:79]\n"
      "v_exp_f32 v89, v89\n"
      "ds_read_b32 v146, %[vchk]\n"
      "v_cvt_pk_bf16_f32 v104, v80, v81\n"
      "v_exp_f32 v90, v90\n"
      "v_cvt_pk_bf16_f32 v105, v82, v83\n"
      "v_exp_f32 v91, v91\n"
      "v_cvt_pk_bf16_f32 v106, v84, v85\n"
      "v_exp_f32 v92, v92\n"
      "v_cvt_pk_bf16_f32 v107, v86, v87\n"
      "v_exp_f32 v93, v93\n"
      "v_cvt_pk_bf16_f32 v108, v88, v89\n"
      "v_exp_f32 v94, v94\n"
      "v_exp_f32 v95, v95\n"
      "s_waitcnt lgkmcnt(0)\n"
      "v_cmp_lt_u32 vcc, v146, v145\n"
      "v_cvt_pk_bf16_f32 v109, v90, v91\n"
      "v_cvt_pk_bf16_f32 v110, v92, v93\n"
      "v_cvt_pk_bf16_f32 v111, v94, v95\n"
      "s_cbranch_vccnz LSPIN0_%=\n"
      "LGO0_%=:\n"
      "v_mfma_f32_32x32x16_bf16 v[200:215], v[128:131], v[104:107], v[200:215]\n"
      "v_exp_f32 v64, v64\n"
      "v_exp_f32 v65, v65\n"
      "v_exp_f32 v66, v66\n"
      "v_mfma_f32_32x32x16_bf16 v[80:95], v[120:123], v[220:223], 0\n"
      "v_exp_f32 v67, v67\n"
      "v_exp_f32 v68, v68\n"
      "v_exp_f32 v69, v69\n"
      "v_mfma_f32_32x32x16_bf16 v[200:215], v[132:135], v[108:111], v[200:215]\n"
      "v_exp_f32 v70, v70\n"
      "v_exp_f32 v71, v71\n"
      "v_exp_f32 v72, v72\n"
      "v_mfma_f32_32x32x8_bf16 v[80:95], v[124:125], v[234:235], v[80:95]\n"
      "v_exp_f32 v73, v73\n"
      "ds_read_b64 v[112:113], %[vk0] offset:21504\n"
      "ds_read_b64 v[114:115], %[vk0] offset:21760\n"
      "ds_read_b64 v[116:117], %[vkt0] offset:22528\n"
      "ds_read_b128 v[128:131], %[vv0] offset:22784\n"
      "ds_read_b128 v[132:135], %[vv0] offset:23456\n"
      "v_cvt_pk_bf16_f32 v96, v64, v65\n"
      "v_exp_f32 v74, v74\n"
      "v_cvt_pk_bf16_f32 v97, v66, v67\n"
      "v_exp_f32 v75, v75\n"
      "v_cvt_pk_bf16_f32 v98, v68, v69\n"
      "v_exp_f32 v76, v76\n"
      "v_cvt_pk_bf16_f32 v99, v70, v71\n"
      "v_exp_f32 v77, v77\n"
      "s_add_u32 m0, %[ldsw], 43008\n"
      "v_cvt_pk_bf16_f32 v100, v72, v73\n"
      "v_exp_f32 v78, v78\n"
      "v_exp_f32 v79, v79\n"
      "v_cvt_pk_bf16_f32 v101, v74, v75\n"
      "v_cvt_pk_bf16_f32 v102, v76, v77\n"
      "v_cvt_pk_bf16_f32 v103, v78, v79\n"
      "v_mfma_f32_32x32x16_bf16 v[152:167], v[136:139], v[96:99], v[152:167]\n"
      "v_exp_f32 v80, v80\n"
      "v_exp_f32 v81, v81\n"
      "v_exp_f32 v82, v82\n"
      "v_mfma_f32_32x32x16_bf16 v[64:79], v[120:123], v[224:227], 0\n"
      "v_exp_f32 v83, v83\n"
      "v_exp_f32 v84, v84\n"
      "v_exp_f32 v85, v85\n"
      "v_mfma_f32_32x32x16_bf16 v[152:167], v[140:143], v[100:103], v[152:167]\n"
      "v_exp_f32 v86, v86\n"
      "v_exp_f32 v87, v87\n"
      "v_exp_f32 v88, v88\n"
      "v_mfma_f32_32x32x8_bf16 v[64:79], v[124:125], v[236:237], v[64:79]\n"
      "v_exp_f32 v89, v89\n"
      "global_load_lds_dwordx4 %[voff], s[40:41] offset:0 sc1\n"
      "v_cvt_pk_bf16_f32 v104, v80, v81\n"
      "v_exp_f32 v90, v90\n"
      "v_cvt_pk_bf16_f32 v105, v82, v83\n"
      "v_exp_f32 v91, v91\n"
      "v_cvt_pk_bf16_f32 v106, v84, v85\n"
      "v_exp_f32 v92, v92\n"
      "v_cvt_pk_bf16_f32 v107, v86, v87\n"
      "v_exp_f32 v93, v93\n"
      "global_load_lds_dwordx4 %[voff], s[40:41] offset:1024 sc1\n"
      "v_cvt_pk_bf16_f32 v108, v88, v89\n"
      "v_exp_f32 v94, v94\n"
      "v_exp_f32 v95, v95\n"
      "v_cvt_pk_bf16_f32 v109, v90, v91\n"
      "v_cvt_pk_bf16_f32 v110, v92, v93\n"
      "v_cvt_pk_bf16_f32 v111, v94, v95\n"
      "v_mfma_f32_32x32x16_bf16 v[168:183], v[136:139], v[104:107], v[168:183]\n"
      "v_exp_f32 v64, v64\n"
      "v_exp_f32 v65, v65\n"
      "v_exp_f32 v66, v66\n"
      "v_mfma_f32_32x32x16_bf16 v[80:95], v[120:123], v[228:231], 0\n"
      "v_exp_f32 v67, v67\n"
      "v_exp_f32 v68, v68\n"
      "v_exp_f32 v69, v69\n"
      "v_mfma_f32_32x32x16_bf16 v[168:183], v[140:143], v[108:111], v[168:183]\n"
      "v_exp_f32 v70, v70\n"
      "v_exp_f32 v71, v71\n"
      "v_exp_f32 v72, v72\n"
      "v_mfma_f32_32x32x8_bf16 v[80:95], v[124:125], v[238:239], v[80:95]\n"
      "v_exp_f32 v73, v73\n"
      "global_load_lds_dwordx4 %[voff], s[40:41] offset:2048 sc1\n"
      "v_cvt_pk_bf16_f32 v96, v64, v65\n"
      "v_exp_f32 v74, v74\n"
      "v_cvt_pk_bf16_f32 v97, v66, v67\n"
      "v_exp_f32 v75, v75\n"
      "v_cvt_pk_bf16_f32 v98, v68, v69\n"
      "v_exp_f32 v76, v76\n"
      "v_cvt_pk_bf16_f32 v99, v70, v71\n"
      "v_exp_f32 v77, v77\n"
      "global_load_lds_dwordx4 %[voff], s[40:41] offset:3072 sc1\n"
      "s_add_u32 m0, m0, 0x1000\n"
      "s_cmp_lg_u32 %[w3], 3\n"
      "v_cvt_pk_bf16_f32 v100, v72, v73\n"
      "v_exp_f32 v78, v78\n"
      "v_exp_f32 v79, v79\n"
      "v_cvt_pk_bf16_f32 v101, v74, v75\n"
      "v_cvt_pk_bf16_f32 v102, v76, v77\n"
      "v_cvt_pk_bf16_f32 v103, v78, v79\n"
      "s_waitcnt lgkmcnt(0)\n"
      "v_mfma_f32_32x32x16_bf16 v[184:199], v[136:139], v[96:99], v[184:199]\n"
      "v_exp_f32 v80, v80\n"
      "v_exp_f32 v81, v81\n"
      "v_exp_f32 v82, v82\n"
      "v_mfma_f32_32x32x16_bf16 v[64:79], v[112:115], v[216:219], 0\n"
      "v_exp_f32 v83, v83\n"
      "v_exp_f32 v84, v84\n"
      "v_exp_f32 v85, v85\n"
      "v_mfma_f32_32x32x16_bf16 v[184:199], v[140:143], v[100:103], v[184:199]\n"
      "v_exp_f32 v86, v86\n"
      "v_exp_f32 v87, v87\n"
      "v_exp_f32 v88, v88\n"
      "v_mfma_f32_32x32x8_bf16 v[64:79], v[116:117], v[232:233], v[64:79]\n"
      "v_exp_f32 v89, v89\n"
      "global_load_lds_dwordx4 v144, s[40:41] offset:0 sc1\n"
      "v_cvt_pk_bf16_f32 v104, v80, v81\n"
      "v_exp_f32 v90, v90\n"
      "v_cvt_pk_bf16_f32 v105, v82, v83\n"
      "v_exp_f32 v91, v91\n"
      "v_cvt_pk_bf16_f32 v106, v84, v85\n"
      "v_exp_f32 v92, v92\n"
      "v_cvt_pk_bf16_f32 v107, v86, v87\n"
      "v_exp_f32 v93, v93\n"
      "s_cbranch_scc1 LSK0_%=\n"
      "global_load_lds_dwordx4 v144, s[40:41] offset:1024 sc1\n"
      "LSK0_%=:\n"
      "s_add_u32 s40, s40, 21504\n"
      "s_addc_u32 s41, s41, 0\n"
      "v_cvt_pk_bf16_f32 v108, v88, v89\n"
      "v_exp_f32 v94, v94\n"
      "v_exp_f32 v95, v95\n"
      "v_cvt_pk_bf16_f32 v109, v90, v91\n"
      "v_cvt_pk_bf16_f32 v110, v92, v93\n"
      "v_cvt_pk_bf16_f32 v111, v94, v95\n"
      "s_mov_b32 s36, 0\n"
      ".p2align 3\n"
      "LOOP_%=:\n"
      "v_mfma_f32_32x32x16_bf16 v[200:215], v[136:139], v[104:107], v[200:215]\n"
      "v_exp_f32 v64, v64\n"
      "v_exp_f32 v65, v65\n"
      "v_exp_f32 v66, v66\n"
      "v_mfma_f32_32x32x16_bf16 v[80:95], v[112:115], v[220:223], 0\n"
      "v_exp_f32 v67, v67\n"
      "v_exp_f32 v68, v68\n"
      "v_exp_f32 v69, v69\n"
      "v_mfma_f32_32x32x16_bf16 v[200:215], v[140:143], v[108:111], v[200:215]\n"
      "v_exp_f32 v70, v70\n"
      "v_exp_f32 v71, v71\n"
      "v_exp_f32 v72, v72\n"
      "v_mfma_f32_32x32x8_bf16 v[80:95], v[116:117], v[234:235], v[80:95]\n"
      "v_exp_f32 v73, v73\n"
      "ds_read_b64 v[120:121], %[vk0] offset:24128\n"
      "ds_read_b64 v[122:123], %[vk0] offset:24384\n"
      "ds_read_b64 v[124:125], %[vkt0] offset:25152\n"
      "ds_read_b128 v[136:139], %[vv0] offset:25408\n"
      "ds_read_b128 v[140:143], %[vv0] offset:26080\n"
      "v_cvt_pk_bf16_f32 v96, v64, v65\n"
      "v_exp_f32 v74, v74\n"
      "v_cvt_pk_bf16_f32 v97, v66, v67\n"
      "v_exp_f32 v75, v75\n"
      "v_cvt_pk_bf16_f32 v98, v68, v69\n"
      "v_exp_f32 v76, v76\n"
      "v_cvt_pk_bf16_f32 v99, v70, v71\n"
      "v_exp_f32 v77, v77\n"
      "v_cvt_pk_bf16_f32 v100, v72, v73\n"
      "v_exp_f32 v78, v78\n"
      "v_exp_f32 v79, v79\n"
      "v_cvt_pk_bf16_f32 v101, v74, v75\n"
      "v_cvt_pk_bf16_f32 v102, v76, v77\n"
      "v_cvt_pk_bf16_f32 v103, v78, v79\n"
      "v_mfma_f32_32x32x16_bf16 v[152:167], v[128:131], v[96:99], v[152:167]\n"
      "v_exp_f32 v80, v80\n"
      "v_exp_f32 v81, v81\n"
      "v_exp_f32 v82, v82\n"
      "v_mfma_f32_32x32x16_bf16 v[64:79], v[112:115], v[224:227], 0\n"
      "v_exp_f32 v83, v83\n"
      "v_exp_f32 v84, v84\n"
      "v_exp_f32 v85, v85\n"
      "v_mfma_f32_32x32x16_bf16 v[152:167], v[132:135], v[100:103], v[152:167]\n"
      "v_exp_f32 v86, v86\n"
      "v_exp_f32 v87, v87\n"
      "v_exp_f32 v88, v88\n"
      "v_mfma_f32_32x32x8_bf16 v[64:79], v[116:117], v[236:237], v[64:79]\n"
      "v_exp_f32 v89, v89\n"
      "v_cvt_pk_bf16_f32 v104, v80, v81\n"
      "v_exp_f32 v90, v90\n"
      "v_cvt_pk_bf16_f32 v105, v82, v83\n"
      "v_exp_f32 v91, v91\n"
      "v_cvt_pk_bf16_f32 v106, v84, v85\n"
      "v_exp_f32 v92, v92\n"
      "v_cvt_pk_bf16_f32 v107, v86, v87\n"
      "v_exp_f32 v93, v93\n"
      "v_cvt_pk_bf16_f32 v108, v88, v89\n"
      "v_exp_f32 v94, v94\n"
      "v_exp_f32 v95, v95\n"
      "v_cvt_pk_bf16_f32 v109, v90, v91\n"
      "v_cvt_pk_bf16_f32 v110, v92, v93\n"
      "v_cvt_pk_bf16_f32 v111, v94, v95\n"
      "v_mfma_f32_32x32x16_bf16 v[168:183], v[128:131], v[104:107], v[168:183]\n"
      "v_exp_f32 v64, v64\n"
      "v_exp_f32 v65, v65\n"
      "v_exp_f32 v66, v66\n"
      "v_mfma_f32_32x32x16_bf16 v[80:95], v[112:115], v[228:231], 0\n"
      "v_exp_f32 v67, v67\n"
      "v_exp_f32 v68, v68\n"
      "v_exp_f32 v69, v69\n"
      "v_mfma_f32_32x32x16_bf16 v[168:183], v[132:135], v[108:111], v[168:183]\n"
      "v_exp_f32 v70, v70\n"
      "v_exp_f32 v71, v71\n"
      "v_exp_f32 v72, v72\n"
      "v_mfma_f32_32x32x8_bf16 v[80:95], v[116:117], v[238:239], v[80:95]\n"
      "v_exp_f32 v73, v73\n"
      "v_cvt_pk_bf16_f32 v96, v64, v65\n"
      "v_exp_f32 v74, v74\n"
      "v_cvt_pk_bf16_f32 v97, v66, v67\n"
      "v_exp_f32 v75, v75\n"
      "v_cvt_pk_bf16_f32 v98, v68, v69\n"
      "v_exp_f32 v76, v76\n"
      "v_cvt_pk_bf16_f32 v99, v70, v71\n"
      "v_exp_f32 v77, v77\n"
      "v_cvt_pk_bf16_f32 v100, v72, v73\n"
      "v_exp_f32 v78, v78\n"
      "v_exp_f32 v79, v79\n"
      "v_cvt_pk_bf16_f32 v101, v74, v75\n"
      "v_cvt_pk_bf16_f32 v102, v76, v77\n"
      "v_cvt_pk_bf16_f32 v103, v78, v79\n"
      "s_waitcnt lgkmcnt(0)\n"
      "v_mfma_f32_32x32x16_bf16 v[184:199], v[128:131], v[96:99], v[184:199]\n"
      "v_exp_f32 v80, v80\n"
      "v_exp_f32 v81, v81\n"
      "v_exp_f32 v82, v82\n"
      "v_mfma_f32_32x32x16_bf16 v[64:79], v[120:123], v[216:219], 0\n"
      "v_exp_f32 v83, v83\n"
      "v_exp_f32 v84, v84\n"
      "v_exp_f32 v85, v85\n"
      "v_mfma_f32_32x32x16_bf16 v[184:199], v[132:135], v[100:103], v[184:199]\n"
      "v_exp_f32 v86, v86\n"
      "v_exp_f32 v87, v87\n"
      "v_exp_f32 v88, v88\n"
      "v_mfma_f32_32x32x8_bf16 v[64:79], v[124:125], v[232:233], v[64:79]\n"
      "v_exp_f32 v89, v89\n"
      "v_cvt_pk_bf16_f32 v104, v80, v81\n"
      "v_exp_f32 v90, v90\n"
      "v_cvt_pk_bf16_f32 v105, v82, v83\n"
      "v_exp_f32 v91, v91\n"
      "v_cvt_pk_bf16_f32 v106, v84, v85\n"
      "v_exp_f32 v92, v92\n"
      "v_cvt_pk_bf16_f32 v107, v86, v87\n"
      "v_exp_f32 v93, v93\n"
      "v_cvt_pk_bf16_f32 v108, v88, v89\n"
      "v_exp_f32 v94, v94\n"
      "v_exp_f32 v95, v95\n"
      "v_cvt_pk_bf16_f32 v109, v90, v91\n"
      "v_cvt_pk_bf16_f32 v110, v92, v93\n"
      "v_cvt_pk_bf16_f32 v111, v94, v95\n"
      "v_mfma_f32_32x32x16_bf16 v[200:215], v[128:131], v[104:107], v[200:215]\n"
      "v_exp_f32 v64, v64\n"
      "v_exp_f32 v65, v65\n"
      "v_exp_f32 v66, v66\n"
      "v_mfma_f32_32x32x16_bf16 v[80:95], v[120:123], v[220:223], 0\n"
      "v_exp_f32 v67, v67\n"
      "v_exp_f32 v68, v68\n"
      "v_exp_f32 v69, v69\n"
      "v_mfma_f32_32x32x16_bf16 v[200:215], v[132:135], v[108:111], v[200:215]\n"
      "v_exp_f32 v70, v70\n"
      "v_exp_f32 v71, v71\n"
      "v_exp_f32 v72, v72\n"
      "v_mfma_f32_32x32x8_bf16 v[80:95], v[124:125], v[234:235], v[80:95]\n"
      "v_exp_f32 v73, v73\n"
      "ds_read_b64 v[112:113], %[vk0] offset:26752\n"
      "ds_read_b64 v[114:115], %[vk0] offset:27008\n"
      "ds_read_b64 v[116:117], %[vkt0] offset:27776\n"
      "ds_read_b128 v[128:131], %[vv0] offset:28032\n"
      "ds_read_b128 v[132:135], %[vv0] offset:28704\n"
      "v_cvt_pk_bf16_f32 v96, v64, v65\n"
      "v_exp_f32 v74, v74\n"
      "v_cvt_pk_bf16_f32 v97, v66, v67\n"
      "v_exp_f32 v75, v75\n"
      "v_cvt_pk_bf16_f32 v98, v68, v69\n"
      "v_exp_f32 v76, v76\n"
      "v_cvt_pk_bf16_f32 v99, v70, v71\n"
      "v_exp_f32 v77, v77\n"
      "v_cvt_pk_bf16_f32 v100, v72, v73\n"
      "v_exp_f32 v78, v78\n"
      "v_exp_f32 v79, v79\n"
      "v_cvt_pk_bf16_f32 v101, v74, v75\n"
      "v_cvt_pk_bf16_f32 v102, v76, v77\n"
      "v_cvt_pk_bf16_f32 v103, v78, v79\n"
      "v_mfma_f32_32x32x16_bf16 v[152:167], v[136:139], v[96:99], v[152:167]\n"
      "v_exp_f32 v80, v80\n"
      "v_exp_f32 v81, v81\n"
      "v_exp_f32 v82, v82\n"
      "v_mfma_f32_32x32x16_bf16 v[64:79], v[120:123], v[224:227], 0\n"
      "v_exp_f32 v83, v83\n"
      "v_exp_f32 v84, v84\n"
      "v_exp_f32 v85, v85\n"
      "v_mfma_f32_32x32x16_bf16 v[152:167], v[140:143], v[100:103], v[152:167]\n"
      "v_exp_f32 v86, v86\n"
      "v_exp_f32 v87, v87\n"
      "v_exp_f32 v88, v88\n"
      "v_mfma_f32_32x32x8_bf16 v[64:79], v[124:125], v[236:237], v[64:79]\n"
      "v_exp_f32 v89, v89\n"
      "v_cvt_pk_bf16_f32 v104, v80, v81\n"
      "v_exp_f32 v90, v90\n"
      "v_cvt_pk_bf16_f32 v105, v82, v83\n"
      "v_exp_f32 v91, v91\n"
      "v_cvt_pk_bf16_f32 v106, v84, v85\n"
      "v_exp_f32 v92, v92\n"
      "v_cvt_pk_bf16_f32 v107, v86, v87\n"
      "v_exp_f32 v93, v93\n"
      "v_cvt_pk_bf16_f32 v108, v88, v89\n"
      "v_exp_f32 v94, v94\n"
      "v_exp_f32 v95, v95\n"
      "v_cvt_pk_bf16_f32 v109, v90, v91\n"
      "v_cvt_pk_bf16_f32 v110, v92, v93\n"
      "v_cvt_pk_bf16_f32 v111, v94, v95\n"
      "v_mfma_f32_32x32x16_bf16 v[168:183], v[136:139], v[104:107], v[168:183]\n"
      "v_exp_f32 v64, v64\n"
      "v_exp_f32 v65, v65\n"
      "v_exp_f32 v66, v66\n"
      "v_mfma_f32_32x32x16_bf16 v[80:95], v[120:123], v[228:231], 0\n"
      "v_exp_f32 v67, v67\n"
      "v_exp_f32 v68, v68\n"
      "v_exp_f32 v69, v69\n"
      "v_mfma_f32_32x32x16_bf16 v[168:183], v[140:143], v[108:111], v[168:183]\n"
      "v_exp_f32 v70, v70\n"
      "v_exp_f32 v71, v71\n"
      "v_exp_f32 v72, v72\n"
      "v_mfma_f32_32x32x8_bf16 v[80:95], v[124:125], v[238:239], v[80:95]\n"
      "v_exp_f32 v73, v73\n"
      "v_cvt_pk_bf16_f32 v96, v64, v65\n"
      "v_exp_f32 v74, v74\n"
      "v_cvt_pk_bf16_f32 v97, v66, v67\n"
      "v_exp_f32 v75, v75\n"
      "v_cvt_pk_bf16_f32 v98, v68, v69\n"
      "v_exp_f32 v76, v76\n"
      "v_cvt_pk_bf16_f32 v99, v70, v71\n"
      "v_exp_f32 v77, v77\n"
      "v_cvt_pk_bf16_f32 v100, v72, v73\n"
      "v_exp_f32 v78, v78\n"
      "v_exp_f32 v79, v79\n"
      "v_cvt_pk_bf16_f32 v101, v74, v75\n"
      "v_cvt_pk_bf16_f32 v102, v76, v77\n"
      "v_cvt_pk_bf16_f32 v103, v78, v79\n"
      "s_waitcnt lgkmcnt(0)\n"
      "v_mfma_f32_32x32x16_bf16 v[184:199], v[136:139], v[96:99], v[184:199]\n"
      "v_exp_f32 v80, v80\n"
      "v_exp_f32 v81, v81\n"
      "v_exp_f32 v82, v82\n"
      "v_mfma_f32_32x32x16_bf16 v[64:79], v[112:115], v[216:219], 0\n"
      "v_exp_f32 v83, v83\n"
      "v_exp_f32 v84, v84\n"
      "v_exp_f32 v85, v85\n"
      "v_mfma_f32_32x32x16_bf16 v[184:199], v[140:143], v[100:103], v[184:199]\n"
      "v_exp_f32 v86, v86\n"
      "v_exp_f32 v87, v87\n"
      "v_exp_f32 v88, v88\n"
      "v_mfma_f32_32x32x8_bf16 v[64:79], v[116:117], v[232:233], v[64:79]\n"
      "v_exp_f32 v89, v89\n"
      "v_cvt_pk_bf16_f32 v104, v80, v81\n"
      "v_exp_f32 v90, v90\n"
      "v_cvt_pk_bf16_f32 v105, v82, v83\n"
      "v_exp_f32 v91, v91\n"
      "v_cvt_pk_bf16_f32 v106, v84, v85\n"
      "v_exp_f32 v92, v92\n"
      "v_cvt_pk_bf16_f32 v107, v86, v87\n"
      "v_exp_f32 v93, v93\n"
      "v_cvt_pk_bf16_f32 v108, v88, v89\n"
      "v_exp_f32 v94, v94\n"
      "v_exp_f32 v95, v95\n"
      "v_cvt_pk_bf16_f32 v109, v90, v91\n"
      "v_cvt_pk_bf16_f32 v110, v92, v93\n"
      "v_cvt_pk_bf16_f32 v111, v94, v95\n"
      "v_mfma_f32_32x32x16_bf16 v[200:215], v[136:139], v[104:107], v[200:215]\n"
      "v_exp_f32 v64, v64\n"
      "v_exp_f32 v65, v65\n"
      "v_exp_f32 v66, v66\n"
      "v_mfma_f32_32x32x16_bf16 v[80:95], v[112:115], v[220:223], 0\n"
      "v_exp_f32 v67, v67\n"
      "v_exp_f32 v68, v68\n"
      "v_exp_f32 v69, v69\n"
      "v_mfma_f32_32x32x16_bf16 v[200:215], v[140:143], v[108:111], v[200:215]\n"
      "v_exp_f32 v70, v70\n"
      "v_exp_f32 v71, v71\n"
      "v_exp_f32 v72, v72\n"
      "v_mfma_f32_32x32x8_bf16 v[80:95], v[116:117], v[234:235], v[80:95]\n"
      "v_exp_f32 v73, v73\n"
      "ds_read_b64 v[120:121], %[vk0] offset:29376\n"
      "ds_read_b64 v[122:123], %[vk0] offset:29632\n"
      "ds_read_b64 v[124:125], %[vkt0] offset:30400\n"
      "ds_read_b128 v[136:139], %[vv0] offset:30656\n"
      "ds_read_b128 v[140:143], %[vv0] offset:31328\n"
      "v_cvt_pk_bf16_f32 v96, v64, v65\n"
      "v_exp_f32 v74, v74\n"
      "v_cvt_pk_bf16_f32 v97, v66, v67\n"
      "v_exp_f32 v75, v75\n"
      "v_cvt_pk_bf16_f32 v98, v68, v69\n"
      "v_exp_f32 v76, v76\n"
      "v_cvt_pk_bf16_f32 v99, v70, v71\n"
      "v_exp_f32 v77, v77\n"
      "v_cvt_pk_bf16_f32 v100, v72, v73\n"
      "v_exp_f32 v78, v78\n"
      "v_exp_f32 v79, v79\n"
      "v_cvt_pk_bf16_f32 v101, v74, v75\n"
      "v_cvt_pk_bf16_f32 v102, v76, v77\n"
      "v_cvt_pk_bf16_f32 v103, v78, v79\n"
      "v_mfma_f32_32x32x16_bf16 v[152:167], v[128:131], v[96:99], v[152:167]\n"
      "v_exp_f32 v80, v80\n"
      "v_exp_f32 v81, v81\n"
      "v_exp_f32 v82, v82\n"
      "v_mfma_f32_32x32x16_bf16 v[64:79], v[112:115], v[224:227], 0\n"
      "v_exp_f32 v83, v83\n"
      "v_exp_f32 v84, v84\n"
      "v_exp_f32 v85, v85\n"
      "v_mfma_f32_32x32x16_bf16 v[152:167], v[132:135], v[100:103], v[152:167]\n"
      "v_exp_f32 v86, v86\n"
      "v_exp_f32 v87, v87\n"
      "v_exp_f32 v88, v88\n"
      "v_mfma_f32_32x32x8_bf16 v[64:79], v[116:117], v[236:237], v[64:79]\n"
      "v_exp_f32 v89, v89\n"
      "v_cvt_pk_bf16_f32 v104, v80, v81\n"
      "v_exp_f32 v90, v90\n"
      "v_cvt_pk_bf16_f32 v105, v82, v83\n"
      "v_exp_f32 v91, v91\n"
      "v_cvt_pk_bf16_f32 v106, v84, v85\n"
      "v_exp_f32 v92, v92\n"
      "v_cvt_pk_bf16_f32 v107, v86, v87\n"
      "v_exp_f32 v93, v93\n"
      "v_cvt_pk_bf16_f32 v108, v88, v89\n"
      "v_exp_f32 v94, v94\n"
      "v_exp_f32 v95, v95\n"
      "v_cvt_pk_bf16_f32 v109, v90, v91\n"
      "v_cvt_pk_bf16_f32 v110, v92, v93\n"
      "v_cvt_pk_bf16_f32 v111, v94, v95\n"
      "v_mfma_f32_32x32x16_bf16 v[168:183], v[128:131], v[104:107], v[168:183]\n"
      "v_exp_f32 v64, v64\n"
      "v_exp_f32 v65, v65\n"
      "v_exp_f32 v66, v66\n"
      "v_mfma_f32_32x32x16_bf16 v[80:95], v[112:115], v[228:231], 0\n"
      "v_exp_f32 v67, v67\n"
      "v_exp_f32 v68, v68\n"
      "v_exp_f32 v69, v69\n"
      "v_mfma_f32_32x32x16_bf16 v[168:183], v[132:135], v[108:111], v[168:183]\n"
      "v_exp_f32 v70, v70\n"
      "v_exp_f32 v71, v71\n"
      "v_exp_f32 v72, v72\n"
      "v_mfma_f32_32x32x8_bf16 v[80:95], v[116:117], v[238:239], v[80:95]\n"
      "v_exp_f32 v73, v73\n"
      "v_add_u32 v145, 1, v145\n"
      "s_waitcnt vmcnt(0) lgkmcnt(0)\n"
      "ds_write_b32 %[vpost], v145\n"
      "v_cvt_pk_bf16_f32 v96, v64, v65\n"
      "v_exp_f32 v74, v74\n"
      "v_cvt_pk_bf16_f32 v97, v66, v67\n"
      "v_exp_f32 v75, v75\n"
      "v_cvt_pk_bf16_f32 v98, v68, v69\n"
      "v_exp_f32 v76, v76\n"
      "v_cvt_pk_bf16_f32 v99, v70, v71\n"
      "v_exp_f32 v77, v77\n"
      "v_cvt_pk_bf16_f32 v100, v72, v73\n"
      "v_exp_f32 v78, v78\n"
      "v_exp_f32 v79, v79\n"
      "v_cvt_pk_bf16_f32 v101, v74, v75\n"
      "v_cvt_pk_bf16_f32 v102, v76, v77\n"
      "v_cvt_pk_bf16_f32 v103, v78, v79\n"
      "v_mfma_f32_32x32x16_bf16 v[184:199], v[128:131], v[96:99], v[184:199]\n"
      "v_exp_f32 v80, v80\n"
      "v_exp_f32 v81, v81\n"
      "v_exp_f32 v82, v82\n"
      "v_mfma_f32_32x32x16_bf16 v[64:79], v[120:123], v[216:219], 0\n"
      "v_exp_f32 v83, v83\n"
      "v_exp_f32 v84, v84\n"
      "v_exp_f32 v85, v85\n"
      "v_mfma_f32_32x32x16_bf16 v[184:199], v[132:135], v[100:103], v[184:199]\n"
      "v_exp_f32 v86, v86\n"
      "v_exp_f32 v87, v87\n"
      "v_exp_f32 v88, v88\n"
      "v_mfma_f32_32x32x8_bf16 v[64:79], v[124:125], v[232:233], v[64:79]\n"
      "v_exp_f32 v89, v89\n"
      "ds_read_b32 v146, %[vchk]\n"
      "v_cvt_pk_bf16_f32 v104, v80, v81\n"
      "v_exp_f32 v90, v90\n"
      "v_cvt_pk_bf16_f32 v105, v82, v83\n"
      "v_exp_f32 v91, v91\n"
      "v_cvt_pk_bf16_f32 v106, v84, v85\n"
      "v_exp_f32 v92, v92\n"
      "v_cvt_pk_bf16_f32 v107, v86, v87\n"
      "v_exp_f32 v93, v93\n"
      "v_cvt_pk_bf16_f32 v108, v88, v89\n"
      "v_exp_f32 v94, v94\n"
      "v_exp_f32 v95, v95\n"
      "s_waitcnt lgkmcnt(0)\n"
      "v_cmp_lt_u32 vcc, v146, v145\n"
      "v_cvt_pk_bf16_f32 v109, v90, v91\n"
      "v_cvt_pk_bf16_f32 v110, v92, v93\n"
      "v_cvt_pk_bf16_f32 v111, v94, v95\n"
      "s_cbranch_vccnz LSPIN1_%=\n"
      "LGO1_%=:\n"
      "v_mfma_f32_32x32x16_bf16 v[200:215], v[128:131], v[104:107], v[200:215]\n"
      "v_exp_f32 v64, v64\n"
      "v_exp_f32 v65, v65\n"
      "v_exp_f32 v66, v66\n"
      "v_mfma_f32_32x32x16_bf16 v[80:95], v[120:123], v[220:223], 0\n"
      "v_exp_f32 v67, v67\n"
      "v_exp_f32 v68, v68\n"
      "v_exp_f32 v69, v69\n"
      "v_mfma_f32_32x32x16_bf16 v[200:215], v[132:135], v[108:111], v[200:215]\n"
      "v_exp_f32 v70, v70\n"
      "v_exp_f32 v71, v71\n"
      "v_exp_f32 v72, v72\n"
      "v_mfma_f32_32x32x8_bf16 v[80:95], v[124:125], v[234:235], v[80:95]\n"
      "v_exp_f32 v73, v73\n"
      "ds_read_b64 v[112:113], %[vk0] offset:43008\n"
      "ds_read_b64 v[114:115], %[vk0] offset:43264\n"
      "ds_read_b64 v[116:117], %[vkt0] offset:44032\n"
      "ds_read_b128 v[128:131], %[vv0] offset:44288\n"
      "ds_read_b128 v[132:135], %[vv0] offset:44960\n"
      "v_cvt_pk_bf16_f32 v96, v64, v65\n"
      "v_exp_f32 v74, v74\n"
      "v_cvt_pk_bf16_f32 v97, v66, v67\n"
      "v_exp_f32 v75, v75\n"
      "v_cvt_pk_bf16_f32 v98, v68, v69\n"
      "v_exp_f32 v76, v76\n"
      "v_cvt_pk_bf16_f32 v99, v70, v71\n"
      "v_exp_f32 v77, v77\n"
      "s_add_u32 m0, %[ldsw], 0\n"
      "v_cvt_pk_bf16_f32 v100, v72, v73\n"
      "v_exp_f32 v78, v78\n"
      "v_exp_f32 v79, v79\n"
      "v_cvt_pk_bf16_f32 v101, v74, v75\n"
      "v_cvt_pk_bf16_f32 v102, v76, v77\n"
      "v_cvt_pk_bf16_f32 v103, v78, v79\n"
      "v_mfma_f32_32x32x16_bf16 v[152:167], v[136:139], v[96:99], v[152:167]\n"
      "v_exp_f32 v80, v80\n"
      "v_exp_f32 v81, v81\n"
      "v_exp_f32 v82, v82\n"
      "v_mfma_f32_32x32x16_bf16 v[64:79], v[120:123], v[224:227], 0\n"
      "v_exp_f32 v83, v83\n"
      "v_exp_f32 v84, v84\n"
      "v_exp_f32 v85, v85\n"
      "v_mfma_f32_32x32x16_bf16 v[152:167], v[140:143], v[100:103], v[152:167]\n"
      "v_exp_f32 v86, v86\n"
      "v_exp_f32 v87, v87\n"
      "v_exp_f32 v88, v88\n"
      "v_mfma_f32_32x32x8_bf16 v[64:79], v[124:125], v[236:237], v[64:79]\n"
      "v_exp_f32 v89, v89\n"
      "global_load_lds_dwordx4 %[voff], s[40:41] offset:0 sc1\n"
      "v_cvt_pk_bf16_f32 v104, v80, v81\n"
      "v_exp_f32 v90, v90\n"
      "v_cvt_pk_bf16_f32 v105, v82, v83\n"
      "v_exp_f32 v91, v91\n"
      "v_cvt_pk_bf16_f32 v106, v84, v85\n"
      "v_exp_f32 v92, v92\n"
      "v_cvt_pk_bf16_f32 v107, v86, v87\n"
      "v_exp_f32 v93, v93\n"
      "global_load_lds_dwordx4 %[voff], s[40:41] offset:1024 sc1\n"
      "v_cvt_pk_bf16_f32 v108, v88, v89\n"
      "v_exp_f32 v94, v94\n"
      "v_exp_f32 v95, v95\n"
      "v_cvt_pk_bf16_f32 v109, v90, v91\n"
      "v_cvt_pk_bf16_f32 v110, v92, v93\n"
      "v_cvt_pk_bf16_f32 v111, v94, v95\n"
      "v_mfma_f32_32x32x16_bf16 v[168:183], v[136:139], v[104:107], v[168:183]\n"
      "v_exp_f32 v64, v64\n"
      "v_exp_f32 v65, v65\n"
      "v_exp_f32 v66, v66\n"
      "v_mfma_f32_32x32x16_bf16 v[80:95], v[120:123], v[228:231], 0\n"
      "v_exp_f32 v67, v67\n"
      "v_exp_f32 v68, v68\n"
      "v_exp_f32 v69, v69\n"
      "v_mfma_f32_32x32x16_bf16 v[168:183], v[140:143], v[108:111], v[168:183]\n"
      "v_exp_f32 v70, v70\n"
      "v_exp_f32 v71, v71\n"
      "v_exp_f32 v72, v72\n"
      "v_mfma_f32_32x32x8_bf16 v[80:95], v[124:125], v[238:239], v[80:95]\n"
      "v_exp_f32 v73, v73\n"
      "global_load_lds_dwordx4 %[voff], s[40:41] offset:2048 sc1\n"
      "v_cvt_pk_bf16_f32 v96, v64, v65\n"
      "v_exp_f32 v74, v74\n"
      "v_cvt_pk_bf16_f32 v97, v66, v67\n"
      "v_exp_f32 v75, v75\n"
      "v_cvt_pk_bf16_f32 v98, v68, v69\n"
      "v_exp_f32 v76, v76\n"
      "v_cvt_pk_bf16_f32 v99, v70, v71\n"
      "v_exp_f32 v77, v77\n"
      "global_load_lds_dwordx4 %[voff], s[40:41] offset:3072 sc1\n"
      "s_add_u32 m0, m0, 0x1000\n"
      "s_cmp_lg_u32 %[w3], 3\n"
      "v_cvt_pk_bf16_f32 v100, v72, v73\n"
      "v_exp_f32 v78, v78\n"
      "v_exp_f32 v79, v79\n"
      "v_cvt_pk_bf16_f32 v101, v74, v75\n"
      "v_cvt_pk_bf16_f32 v102, v76, v77\n"
      "v_cvt_pk_bf16_f32 v103, v78, v79\n"
      "s_waitcnt lgkmcnt(0)\n"
      "v_mfma_f32_32x32x16_bf16 v[184:199], v[136:139], v[96:99], v[184:199]\n"
      "v_exp_f32 v80, v80\n"
      "v_exp_f32 v81, v81\n"
      "v_exp_f32 v82, v82\n"
      "v_mfma_f32_32x32x16_bf16 v[64:79], v[112:115], v[216:219], 0\n"
      "v_exp_f32 v83, v83\n"
      "v_exp_f32 v84, v84\n"
      "v_exp_f32 v85, v85\n"
      "v_mfma_f32_32x32x16_bf16 v[184:199], v[140:143], v[100:103], v[184:199]\n"
      "v_exp_f32 v86, v86\n"
      "v_exp_f32 v87, v87\n"
      "v_exp_f32 v88, v88\n"
      "v_mfma_f32_32x32x8_bf16 v[64:79], v[116:117], v[232:233], v[64:79]\n"
      "v_exp_f32 v89, v89\n"
      "global_load_lds_dwordx4 v144, s[40:41] offset:0 sc1\n"
      "v_cvt_pk_bf16_f32 v104, v80, v81\n"
      "v_exp_f32 v90, v90\n"
      "v_cvt_pk_bf16_f32 v105, v82, v83\n"
      "v_exp_f32 v91, v91\n"
      "v_cvt_pk_bf16_f32 v106, v84, v85\n"
      "v_exp_f32 v92, v92\n"
      "v_cvt_pk_bf16_f32 v107, v86, v87\n"
      "v_exp_f32 v93, v93\n"
      "s_cbranch_scc1 LSK1_%=\n"
      "global_load_lds_dwordx4 v144, s[40:41] offset:1024 sc1\n"
      "LSK1_%=:\n"
      "s_add_u32 s40, s40, 21504\n"
      "s_addc_u32 s41, s41, 0\n"
      "v_cvt_pk_bf16_f32 v108, v88, v89\n"
      "v_exp_f32 v94, v94\n"
      "v_exp_f32 v95, v95\n"
      "v_cvt_pk_bf16_f32 v109, v90, v91\n"
      "v_cvt_pk_bf16_f32 v110, v92, v93\n"
      "v_cvt_pk_bf16_f32 v111, v94, v95\n"
      "v_mfma_f32_32x32x16_bf16 v[200:215], v[136:139], v[104:107], v[200:215]\n"
      "v_exp_f32 v64, v64\n"
      "v_exp_f32 v65, v65\n"
      "v_exp_f32 v66, v66\n"
      "v_mfma_f32_32x32x16_bf16 v[80:95], v[112:115], v[220:223], 0\n"
      "v_exp_f32 v67, v67\n"
      "v_exp_f32 v68, v68\n"
      "v_exp_f32 v69, v69\n"
      "v_mfma_f32_32x32x16_bf16 v[200:215], v[140:143], v[108:111], v[200:215]\n"
      "v_exp_f32 v70, v70\n"
      "v_exp_f32 v71, v71\n"
      "v_exp_f32 v72, v72\n"
      "v_mfma_f32_32x32x8_bf16 v[80:95], v[116:117], v[234:235], v[80:95]\n"
      "v_exp_f32 v73, v73\n"
      "ds_read_b64 v[120:121], %[vk0] offset:45632\n"
      "ds_read_b64 v[122:123], %[vk0] offset:45888\n"
      "ds_read_b64 v[124:125], %[vkt0] offset:46656\n"
      "ds_read_b128 v[136:139], %[vv0] offset:46912\n"
      "ds_read_b128 v[140:143], %[vv0] offset:47584\n"
      "v_cvt_pk_bf16_f32 v96, v64, v65\n"
      "v_exp_f32 v74, v74\n"
      "v_cvt_pk_bf16_f32 v97, v66, v67\n"
      "v_exp_f32 v75, v75\n"
      "v_cvt_pk_bf16_f32 v98, v68, v69\n"
      "v_exp_f32 v76, v76\n"
      "v_cvt_pk_bf16_f32 v99, v70, v71\n"
      "v_exp_f32 v77, v77\n"
      "v_cvt_pk_bf16_f32 v100, v72, v73\n"
      "v_exp_f32 v78, v78\n"
      "v_exp_f32 v79, v79\n"
      "v_cvt_pk_bf16_f32 v101, v74, v75\n"
      "v_cvt_pk_bf16_f32 v102, v76, v77\n"
      "v_cvt_pk_bf16_f32 v103, v78, v79\n"
      "v_mfma_f32_32x32x16_bf16 v[152:167], v[128:131], v[96:99], v[152:167]\n"
      "v_exp_f32 v80, v80\n"
      "v_exp_f32 v81, v81\n"
      "v_exp_f32 v82, v82\n"
      "v_mfma_f32_32x32x16_bf16 v[64:79], v[112:115], v[224:227], 0\n"
      "v_exp_f32 v83, v83\n"
      "v_exp_f32 v84, v84\n"
      "v_exp_f32 v85, v85\n"
      "v_mfma_f32_32x32x16_bf16 v[152:167], v[132:135], v[100:103], v[152:167]\n"
      "v_exp_f32 v86, v86\n"
      "v_exp_f32 v87, v87\n"
      "v_exp_f32 v88, v88\n"
      "v_mfma_f32_32x32x8_bf16 v[64:79], v[116:117], v[236:237], v[64:79]\n"
      "v_exp_f32 v89, v89\n"
      "v_cvt_pk_bf16_f32 v104, v80, v81\n"
      "v_exp_f32 v90, v90\n"
      "v_cvt_pk_bf16_f32 v105, v82, v83\n"
      "v_exp_f32 v91, v91\n"
      "v_cvt_pk_bf16_f32 v106, v84, v85\n"
      "v_exp_f32 v92, v92\n"
      "v_cvt_pk_bf16_f32 v107, v86, v87\n"
      "v_exp_f32 v93, v93\n"
      "v_cvt_pk_bf16_f32 v108, v88, v89\n"
      "v_exp_f32 v94, v94\n"
      "v_exp_f32 v95, v95\n"
      "v_cvt_pk_bf16_f32 v109, v90, v91\n"
      "v_cvt_pk_bf16_f32 v110, v92, v93\n"
      "v_cvt_pk_bf16_f32 v111, v94, v95\n"
      "v_mfma_f32_32x32x16_bf16 v[168:183], v[128:131], v[104:107], v[168:183]\n"
      "v_exp_f32 v64, v64\n"
      "v_exp_f32 v65, v65\n"
      "v_exp_f32 v66, v66\n"
      "v_mfma_f32_32x32x16_bf16 v[80:95], v[112:115], v[228:231], 0\n"
      "v_exp_f32 v67, v67\n"
      "v_exp_f32 v68, v68\n"
      "v_exp_f32 v69, v69\n"
      "v_mfma_f32_32x32x16_bf16 v[168:183], v[132:135], v[108:111], v[168:183]\n"
      "v_exp_f32 v70, v70\n"
      "v_exp_f32 v71, v71\n"
      "v_exp_f32 v72, v72\n"
      "v_mfma_f32_32x32x8_bf16 v[80:95], v[116:117], v[238:239], v[80:95]\n"
      "v_exp_f32 v73, v73\n"
      "v_cvt_pk_bf16_f32 v96, v64, v65\n"
      "v_exp_f32 v74, v74\n"
      "v_cvt_pk_bf16_f32 v97, v66, v67\n"
      "v_exp_f32 v75, v75\n"
      "v_cvt_pk_bf16_f32 v98, v68, v69\n"
      "v_exp_f32 v76, v76\n"
      "v_cvt_pk_bf16_f32 v99, v70, v71\n"
      "v_exp_f32 v77, v77\n"
      "v_cvt_pk_bf16_f32 v100, v72, v73\n"
      "v_exp_f32 v78, v78\n"
      "v_exp_f32 v79, v79\n"
      "v_cvt_pk_bf16_f32 v101, v74, v75\n"
      "v_cvt_pk_bf16_f32 v102, v76, v77\n"
      "v_cvt_pk_bf16_f32 v103, v78, v79\n"
      "s_waitcnt lgkmcnt(0)\n"
      "v_mfma_f32_32x32x16_bf16 v[184:199], v[128:131], v[96:99], v[184:199]\n"
      "v_exp_f32 v80, v80\n"
      "v_exp_f32 v81, v81\n"
      "v_exp_f32 v82, v82\n"
      "v_mfma_f32_32x32x16_bf16 v[64:79], v[120:123], v[216:219], 0\n"
      "v_exp_f32 v83, v83\n"
      "v_exp_f32 v84, v84\n"
      "v_exp_f32 v85, v85\n"
      "v_mfma_f32_32x32x16_bf16 v[184:199], v[132:135], v[100:103], v[184:199]\n"
      "v_exp_f32 v86, v86\n"
      "v_exp_f32 v87, v87\n"
      "v_exp_f32 v88, v88\n"
      "v_mfma_f32_32x32x8_bf16 v[64:79], v[124:125], v[232:233], v[64:79]\n"
      "v_exp_f32 v89, v89\n"
      "v_cvt_pk_bf16_f32 v104, v80, v81\n"
      "v_exp_f32 v90, v90\n"
      "v_cvt_pk_bf16_f32 v105, v82, v83\n"
      "v_exp_f32 v91, v91\n"
      "v_cvt_pk_bf16_f32 v106, v84, v85\n"
      "v_exp_f32 v92, v92\n"
      "v_cvt_pk_bf16_f32 v107, v86, v87\n"
      "v_exp_f32 v93, v93\n"
      "v_cvt_pk_bf16_f32 v108, v88, v89\n"
      "v_exp_f32 v94, v94\n"
      "v_exp_f32 v95, v95\n"
      "v_cvt_pk_bf16_f32 v109, v90, v91\n"
      "v_cvt_pk_bf16_f32 v110, v92, v93\n"
      "v_cvt_pk_bf16_f32 v111, v94, v95\n"
      "v_mfma_f32_32x32x16_bf16 v[200:215], v[128:131], v[104:107], v[200:215]\n"
      "v_exp_f32 v64, v64\n"
      "v_exp_f32 v65, v65\n"
      "v_exp_f32 v66, v66\n"
      "v_mfma_f32_32x32x16_bf16 v[80:95], v[120:123], v[220:223], 0\n"
      "v_exp_f32 v67, v67\n"
      "v_exp_f32 v68, v68\n"
      "v_exp_f32 v69, v69\n"
      "v_mfma_f32_32x32x16_bf16 v[200:215], v[132:135], v[108:111], v[200:215]\n"
      "v_exp_f32 v70, v70\n"
      "v_exp_f32 v71, v71\n"
      "v_exp_f32 v72, v72\n"
      "v_mfma_f32_32x32x8_bf16 v[80:95], v[124:125], v[234:235], v[80:95]\n"
      "v_exp_f32 v73, v73\n"
      "ds_read_b64 v[112:113], %[vk0] offset:48256\n"
      "ds_read_b64 v[114:115], %[vk0] offset:48512\n"
      "ds_read_b64 v[116:117], %[vkt0] offset:49280\n"
      "ds_read_b128 v[128:131], %[vv0] offset:49536\n"
      "ds_read_b128 v[132:135], %[vv0] offset:50208\n"
      "v_cvt_pk_bf16_f32 v96, v64, v65\n"
      "v_exp_f32 v74, v74\n"
      "v_cvt_pk_bf16_f32 v97, v66, v67\n"
      "v_exp_f32 v75, v75\n"
      "v_cvt_pk_bf16_f32 v98, v68, v69\n"
      "v_exp_f32 v76, v76\n"
      "v_cvt_pk_bf16_f32 v99, v70, v71\n"
      "v_exp_f32 v77, v77\n"
      "v_cvt_pk_bf16_f32 v100, v72, v73\n"
      "v_exp_f32 v78, v78\n"
      "v_exp_f32 v79, v79\n"
      "v_cvt_pk_bf16_f32 v101, v74, v75\n"
      "v_cvt_pk_bf16_f32 v102, v76, v77\n"
      "v_cvt_pk_bf16_f32 v103, v78, v79\n"
      "v_mfma_f32_32x32x16_bf16 v[152:167], v[136:139], v[96:99], v[152:167]\n"
      "v_exp_f32 v80, v80\n"
      "v_exp_f32 v81, v81\n"
      "v_exp_f32 v82, v82\n"
      "v_mfma_f32_32x32x16_bf16 v[64:79], v[120:123], v[224:227], 0\n"
      "v_exp_f32 v83, v83\n"
      "v_exp_f32 v84, v84\n"
      "v_exp_f32 v85, v85\n"
      "v_mfma_f32_32x32x16_bf16 v[152:167], v[140:143], v[100:103], v[152:167]\n"
      "v_exp_f32 v86, v86\n"
      "v_exp_f32 v87, v87\n"
      "v_exp_f32 v88, v88\n"
      "v_mfma_f32_32x32x8_bf16 v[64:79], v[124:125], v[236:237], v[64:79]\n"
      "v_exp_f32 v89, v89\n"
      "v_cvt_pk_bf16_f32 v104, v80, v81\n"
      "v_exp_f32 v90, v90\n"
      "v_cvt_pk_bf16_f32 v105, v82, v83\n"
      "v_exp_f32 v91, v91\n"
      "v_cvt_pk_bf16_f32 v106, v84, v85\n"
      "v_exp_f32 v92, v92\n"
      "v_cvt_pk_bf16_f32 v107, v86, v87\n"
      "v_exp_f32 v93, v93\n"
      "v_cvt_pk_bf16_f32 v108, v88, v89\n"
      "v_exp_f32 v94, v94\n"
      "v_exp_f32 v95, v95\n"
      "v_cvt_pk_bf16_f32 v109, v90, v91\n"
      "v_cvt_pk_bf16_f32 v110, v92, v93\n"
      "v_cvt_pk_bf16_f32 v111, v94, v95\n"
      "v_mfma_f32_32x32x16_bf16 v[168:183], v[136:139], v[104:107], v[168:183]\n"
      "v_exp_f32 v64, v64\n"
      "v_exp_f32 v65, v65\n"
      "v_exp_f32 v66, v66\n"
      "v_mfma_f32_32x32x16_bf16 v[80:95], v[120:123], v[228:231], 0\n"
      "v_exp_f32 v67, v67\n"
      "v_exp_f32 v68, v68\n"
      "v_exp_f32 v69, v69\n"
      "v_mfma_f32_32x32x16_bf16 v[168:183], v[140:143], v[108:111], v[168:183]\n"
      "v_exp_f32 v70, v70\n"
      "v_exp_f32 v71, v71\n"
      "v_exp_f32 v72, v72\n"
      "v_mfma_f32_32x32x8_bf16 v[80:95], v[124:125], v[238:239], v[80:95]\n"
      "v_exp_f32 v73, v73\n"
      "v_cvt_pk_bf16_f32 v96, v64, v65\n"
      "v_exp_f32 v74, v74\n"
      "v_cvt_pk_bf16_f32 v97, v66, v67\n"
      "v_exp_f32 v75, v75\n"
      "v_cvt_pk_bf16_f32 v98, v68, v69\n"
      "v_exp_f32 v76, v76\n"
      "v_cvt_pk_bf16_f32 v99, v70, v71\n"
      "v_exp_f32 v77, v77\n"
      "v_cvt_pk_bf16_f32 v100, v72, v73\n"
      "v_exp_f32 v78, v78\n"
      "v_exp_f32 v79, v79\n"
      "v_cvt_pk_bf16_f32 v101, v74, v75\n"
      "v_cvt_pk_bf16_f32 v102, v76, v77\n"
      "v_cvt_pk_bf16_f32 v103, v78, v79\n"
      "s_waitcnt lgkmcnt(0)\n"
      "v_mfma_f32_32x32x16_bf16 v[184:199], v[136:139], v[96:99], v[184:199]\n"
      "v_exp_f32 v80, v80\n"
      "v_exp_f32 v81, v81\n"
      "v_exp_f32 v82, v82\n"
      "v_mfma_f32_32x32x16_bf16 v[64:79], v[112:115], v[216:219], 0\n"
      "v_exp_f32 v83, v83\n"
      "v_exp_f32 v84, v84\n"
      "v_exp_f32 v85, v85\n"
      "v_mfma_f32_32x32x16_bf16 v[184:199], v[140:143], v[100:103], v[184:199]\n"
      "v_exp_f32 v86, v86\n"
      "v_exp_f32 v87, v87\n"
      "v_exp_f32 v88, v88\n"
      "v_mfma_f32_32x32x8_bf16 v[64:79], v[116:117], v[232:233], v[64:79]\n"
      "v_exp_f32 v89, v89\n"
      "v_cvt_pk_bf16_f32 v104, v80, v81\n"
      "v_exp_f32 v90, v90\n"
      "v_cvt_pk_bf16_f32 v105, v82, v83\n"
      "v_exp_f32 v91, v91\n"
      "v_cvt_pk_bf16_f32 v106, v84, v85\n"
      "v_exp_f32 v92, v92\n"
      "v_cvt_pk_bf16_f32 v107, v86, v87\n"
      "v_exp_f32 v93, v93\n"
      "v_cvt_pk_bf16_f32 v108, v88, v89\n"
      "v_exp_f32 v94, v94\n"
      "v_exp_f32 v95, v95\n"
      "v_cvt_pk_bf16_f32 v109, v90, v91\n"
      "v_cvt_pk_bf16_f32 v110, v92, v93\n"
      "v_cvt_pk_bf16_f32 v111, v94, v95\n"
      "v_mfma_f32_32x32x16_bf16 v[200:215], v[136:139], v[104:107], v[200:215]\n"
      "v_exp_f32 v64, v64\n"
      "v_exp_f32 v65, v65\n"
      "v_exp_f32 v66, v66\n"
      "v_mfma_f32_32x32x16_bf16 v[80:95], v[112:115], v[220:223], 0\n"
      "v_exp_f32 v67, v67\n"
      "v_exp_f32 v68, v68\n"
      "v_exp_f32 v69, v69\n"
      "v_mfma_f32_32x32x16_bf16 v[200:215], v[140:143], v[108:111], v[200:215]\n"
      "v_exp_f32 v70, v70\n"
      "v_exp_f32 v71, v71\n"
      "v_exp_f32 v72, v72\n"
      "v_mfma_f32_32x32x8_bf16 v[80:95], v[116:117], v[234:235], v[80:95]\n"
      "v_exp_f32 v73, v73\n"
      "ds_read_b64 v[120:121], %[vk0] offset:50880\n"
      "ds_read_b64 v[122:123], %[vk0] offset:51136\n"
      "ds_read_b64 v[124:125], %[vkt0] offset:51904\n"
      "ds_read_b128 v[136:139], %[vv0] offset:52160\n"
      "ds_read_b128 v[140:143], %[vv0] offset:52832\n"
      "v_cvt_pk_bf16_f32 v96, v64, v65\n"
      "v_exp_f32 v74, v74\n"
      "v_cvt_pk_bf16_f32 v97, v66, v67\n"
      "v_exp_f32 v75, v75\n"
      "v_cvt_pk_bf16_f32 v98, v68, v69\n"
      "v_exp_f32 v76, v76\n"
      "v_cvt_pk_bf16_f32 v99, v70, v71\n"
      "v_exp_f32 v77, v77\n"
      "v_cvt_pk_bf16_f32 v100, v72, v73\n"
      "v_exp_f32 v78, v78\n"
      "v_exp_f32 v79, v79\n"
      "v_cvt_pk_bf16_f32 v101, v74, v75\n"
      "v_cvt_pk_bf16_f32 v102, v76, v77\n"
      "v_cvt_pk_bf16_f32 v103, v78, v79\n"
      "v_mfma_f32_32x32x16_bf16 v[152:167], v[128:131], v[96:99], v[152:167]\n"
      "v_exp_f32 v80, v80\n"
      "v_exp_f32 v81, v81\n"
      "v_exp_f32 v82, v82\n"
      "v_mfma_f32_32x32x16_bf16 v[64:79], v[112:115], v[224:227], 0\n"
      "v_exp_f32 v83, v83\n"
      "v_exp_f32 v84, v84\n"
      "v_exp_f32 v85, v85\n"
      "v_mfma_f32_32x32x16_bf16 v[152:167], v[132:135], v[100:103], v[152:167]\n"
      "v_exp_f32 v86, v86\n"
      "v_exp_f32 v87, v87\n"
      "v_exp_f32 v88, v88\n"
      "v_mfma_f32_32x32x8_bf16 v[64:79], v[116:117], v[236:237], v[64:79]\n"
      "v_exp_f32 v89, v89\n"
      "v_cvt_pk_bf16_f32 v104, v80, v81\n"
      "v_exp_f32 v90, v90\n"
      "v_cvt_pk_bf16_f32 v105, v82, v83\n"
      "v_exp_f32 v91, v91\n"
      "v_cvt_pk_bf16_f32 v106, v84, v85\n"
      "v_exp_f32 v92, v92\n"
      "v_cvt_pk_bf16_f32 v107, v86, v87\n"
      "v_exp_f32 v93, v93\n"
      "v_cvt_pk_bf16_f32 v108, v88, v89\n"
      "v_exp_f32 v94, v94\n"
      "v_exp_f32 v95, v95\n"
      "v_cvt_pk_bf16_f32 v109, v90, v91\n"
      "v_cvt_pk_bf16_f32 v110, v92, v93\n"
      "v_cvt_pk_bf16_f32 v111, v94, v95\n"
      "v_mfma_f32_32x32x16_bf16 v[168:183], v[128:131], v[104:107], v[168:183]\n"
      "v_exp_f32 v64, v64\n"
      "v_exp_f32 v65, v65\n"
      "v_exp_f32 v66, v66\n"
      "v_mfma_f32_32x32x16_bf16 v[80:95], v[112:115], v[228:231], 0\n"
      "v_exp_f32 v67, v67\n"
      "v_exp_f32 v68, v68\n"
      "v_exp_f32 v69, v69\n"
      "v_mfma_f32_32x32x16_bf16 v[168:183], v[132:135], v[108:111], v[168:183]\n"
      "v_exp_f32 v70, v70\n"
      "v_exp_f32 v71, v71\n"
      "v_exp_f32 v72, v72\n"
      "v_mfma_f32_32x32x8_bf16 v[80:95], v[116:117], v[238:239], v[80:95]\n"
      "v_exp_f32 v73, v73\n"
      "v_add_u32 v145, 1, v145\n"
      "s_waitcnt vmcnt(0) lgkmcnt(0)\n"
      "ds_write_b32 %[vpost], v145\n"
      "v_cvt_pk_bf16_f32 v96, v64, v65\n"
      "v_exp_f32 v74, v74\n"
      "v_cvt_pk_bf16_f32 v97, v66, v67\n"
      "v_exp_f32 v75, v75\n"
      "v_cvt_pk_bf16_f32 v98, v68, v69\n"
      "v_exp_f32 v76, v76\n"
      "v_cvt_pk_bf16_f32 v99, v70, v71\n"
      "v_exp_f32 v77, v77\n"
      "v_cvt_pk_bf16_f32 v100, v72, v73\n"
      "v_exp_f32 v78, v78\n"
      "v_exp_f32 v79, v79\n"
      "v_cvt_pk_bf16_f32 v101, v74, v75\n"
      "v_cvt_pk_bf16_f32 v102, v76, v77\n"
      "v_cvt_pk_bf16_f32 v103, v78, v79\n"
      "v_mfma_f32_32x32x16_bf16 v[184:199], v[128:131], v[96:99], v[184:199]\n"
      "v_exp_f32 v80, v80\n"
      "v_exp_f32 v81, v81\n"
      "v_exp_f32 v82, v82\n"
      "v_mfma_f32_32x32x16_bf16 v[64:79], v[120:123], v[216:219], 0\n"
      "v_exp_f32 v83, v83\n"
      "v_exp_f32 v84, v84\n"
      "v_exp_f32 v85, v85\n"
      "v_mfma_f32_32x32x16_bf16 v[184:199], v[132:135], v[100:103], v[184:199]\n"
      "v_exp_f32 v86, v86\n"
      "v_exp_f32 v87, v87\n"
      "v_exp_f32 v88, v88\n"
      "v_mfma_f32_32x32x8_bf16 v[64:79], v[124:125], v[232:233], v[64:79]\n"
      "v_exp_f32 v89, v89\n"
      "ds_read_b32 v146, %[vchk]\n"
      "v_cvt_pk_bf16_f32 v104, v80, v81\n"
      "v_exp_f32 v90, v90\n"
      "v_cvt_pk_bf16_f32 v105, v82, v83\n"
      "v_exp_f32 v91, v91\n"
      "v_cvt_pk_bf16_f32 v106, v84, v85\n"
      "v_exp_f32 v92, v92\n"
      "v_cvt_pk_bf16_f32 v107, v86, v87\n"
      "v_exp_f32 v93, v93\n"
      "v_cvt_pk_bf16_f32 v108, v88, v89\n"
      "v_exp_f32 v94, v94\n"
      "v_exp_f32 v95, v95\n"
      "s_waitcnt lgkmcnt(0)\n"
      "v_cmp_lt_u32 vcc, v146, v145\n"
      "v_cvt_pk_bf16_f32 v109, v90, v91\n"
      "v_cvt_pk_bf16_f32 v110, v92, v93\n"
      "v_cvt_pk_bf16_f32 v111, v94, v95\n"
      "s_cbranch_vccnz LSPIN2_%=\n"
      "LGO2_%=:\n"
      "v_mfma_f32_32x32x16_bf16 v[200:215], v[128:131], v[104:107], v[200:215]\n"
      "v_exp_f32 v64, v64\n"
      "v_exp_f32 v65, v65\n"
      "v_exp_f32 v66, v66\n"
      "v_mfma_f32_32x32x16_bf16 v[80:95], v[120:123], v[220:223], 0\n"
      "v_exp_f32 v67, v67\n"
      "v_exp_f32 v68, v68\n"
      "v_exp_f32 v69, v69\n"
      "v_mfma_f32_32x32x16_bf16 v[200:215], v[132:135], v[108:111], v[200:215]\n"
      "v_exp_f32 v70, v70\n"
      "v_exp_f32 v71, v71\n"
      "v_exp_f32 v72, v72\n"
      "v_mfma_f32_32x32x8_bf16 v[80:95], v[124:125], v[234:235], v[80:95]\n"
      "v_exp_f32 v73, v73\n"
      "ds_read_b64 v[112:113], %[vk0] offset:0\n"
      "ds_read_b64 v[114:115], %[vk0] offset:256\n"
      "ds_read_b64 v[116:117], %[vkt0] offset:1024\n"
      "ds_read_b128 v[128:131], %[vv0] offset:1280\n"
      "ds_read_b128 v[132:135], %[vv0] offset:1952\n"
      "v_cvt_pk_bf16_f32 v96, v64, v65\n"
      "v_exp_f32 v74, v74\n"
      "v_cvt_pk_bf16_f32 v97, v66, v67\n"
      "v_exp_f32 v75, v75\n"
      "v_cvt_pk_bf16_f32 v98, v68, v69\n"
      "v_exp_f32 v76, v76\n"
      "v_cvt_pk_bf16_f32 v99, v70, v71\n"
      "v_exp_f32 v77, v77\n"
      "s_add_u32 m0, %[ldsw], 21504\n"
      "v_cvt_pk_bf16_f32 v100, v72, v73\n"
      "v_exp_f32 v78, v78\n"
      "v_exp_f32 v79, v79\n"
      "v_cvt_pk_bf16_f32 v101, v74, v75\n"
      "v_cvt_pk_bf16_f32 v102, v76, v77\n"
      "v_cvt_pk_bf16_f32 v103, v78, v79\n"
      "v_mfma_f32_32x32x16_bf16 v[152:167], v[136:139], v[96:99], v[152:167]\n"
      "v_exp_f32 v80, v80\n"
      "v_exp_f32 v81, v81\n"
      "v_exp_f32 v82, v82\n"
      "v_mfma_f32_32x32x16_bf16 v[64:79], v[120:123], v[224:227], 0\n"
      "v_exp_f32 v83, v83\n"
      "v_exp_f32 v84, v84\n"
      "v_exp_f32 v85, v85\n"
      "v_mfma_f32_32x32x16_bf16 v[152:167], v[140:143], v[100:103], v[152:167]\n"
      "v_exp_f32 v86, v86\n"
      "v_exp_f32 v87, v87\n"
      "v_exp_f32 v88, v88\n"
      "v_mfma_f32_32x32x8_bf16 v[64:79], v[124:125], v[236:237], v[64:79]\n"
      "v_exp_f32 v89, v89\n"
      "global_load_lds_dwordx4 %[voff], s[40:41] offset:0 sc1\n"
      "v_cvt_pk_bf16_f32 v104, v80, v81\n"
      "v_exp_f32 v90, v90\n"
      "v_cvt_pk_bf16_f32 v105, v82, v83\n"
      "v_exp_f32 v91, v91\n"
      "v_cvt_pk_bf16_f32 v106, v84, v85\n"
      "v_exp_f32 v92, v92\n"
      "v_cvt_pk_bf16_f32 v107, v86, v87\n"
      "v_exp_f32 v93, v93\n"
      "global_load_lds_dwordx4 %[voff], s[40:41] offset:1024 sc1\n"
      "v_cvt_pk_bf16_f32 v108, v88, v89\n"
      "v_exp_f32 v94, v94\n"
      "v_exp_f32 v95, v95\n"
      "v_cvt_pk_bf16_f32 v109, v90, v91\n"
      "v_cvt_pk_bf16_f32 v110, v92, v93\n"
      "v_cvt_pk_bf16_f32 v111, v94, v95\n"
      "v_mfma_f32_32x32x16_bf16 v[168:183], v[136:139], v[104:107], v[168:183]\n"
      "v_exp_f32 v64, v64\n"
      "v_exp_f32 v65, v65\n"
      "v_exp_f32 v66, v66\n"
      "v_mfma_f32_32x32x16_bf16 v[80:95], v[120:123], v[228:231], 0\n"
      "v_exp_f32 v67, v67\n"
      "v_exp_f32 v68, v68\n"
      "v_exp_f32 v69, v69\n"
      "v_mfma_f32_32x32x16_bf16 v[168:183], v[140:143], v[108:111], v[168:183]\n"
      "v_exp_f32 v70, v70\n"
      "v_exp_f32 v71, v71\n"
      "v_exp_f32 v72, v72\n"
      "v_mfma_f32_32x32x8_bf16 v[80:95], v[124:125], v[238:239], v[80:95]\n"
      "v_exp_f32 v73, v73\n"
      "global_load_lds_dwordx4 %[voff], s[40:41] offset:2048 sc1\n"
      "v_cvt_pk_bf16_f32 v96, v64, v65\n"
      "v_exp_f32 v74, v74\n"
      "v_cvt_pk_bf16_f32 v97, v66, v67\n"
      "v_exp_f32 v75, v75\n"
      "v_cvt_pk_bf16_f32 v98, v68, v69\n"
      "v_exp_f32 v76, v76\n"
      "v_cvt_pk_bf16_f32 v99, v70, v71\n"
      "v_exp_f32 v77, v77\n"
      "global_load_lds_dwordx4 %[voff], s[40:41] offset:3072 sc1\n"
      "s_add_u32 m0, m0, 0x1000\n"
      "s_cmp_lg_u32 %[w3], 3\n"
      "v_cvt_pk_bf16_f32 v100, v72, v73\n"
      "v_exp_f32 v78, v78\n"
      "v_exp_f32 v79, v79\n"
      "v_cvt_pk_bf16_f32 v101, v74, v75\n"
      "v_cvt_pk_bf16_f32 v102, v76, v77\n"
      "v_cvt_pk_bf16_f32 v103, v78, v79\n"
      "s_waitcnt lgkmcnt(0)\n"
      "v_mfma_f32_32x32x16_bf16 v[184:199], v[136:139], v[96:99], v[184:199]\n"
      "v_exp_f32 v80, v80\n"
      "v_exp_f32 v81, v81\n"
      "v_exp_f32 v82, v82\n"
      "v_mfma_f32_32x32x16_bf16 v[64:79], v[112:115], v[216:219], 0\n"
      "v_exp_f32 v83, v83\n"
      "v_exp_f32 v84, v84\n"
      "v_exp_f32 v85, v85\n"
      "v_mfma_f32_32x32x16_bf16 v[184:199], v[140:143], v[100:103], v[184:199]\n"
      "v_exp_f32 v86, v86\n"
      "v_exp_f32 v87, v87\n"
      "v_exp_f32 v88, v88\n"
      "v_mfma_f32_32x32x8_bf16 v[64:79], v[116:117], v[232:233], v[64:79]\n"
      "v_exp_f32 v89, v89\n"
      "global_load_lds_dwordx4 v144, s[40:41] offset:0 sc1\n"
      "v_cvt_pk_bf16_f32 v104, v80, v81\n"
      "v_exp_f32 v90, v90\n"
      "v_cvt_pk_bf16_f32 v105, v82, v83\n"
      "v_exp_f32 v91, v91\n"
      "v_cvt_pk_bf16_f32 v106, v84, v85\n"
      "v_exp_f32 v92, v92\n"
      "v_cvt_pk_bf16_f32 v107, v86, v87\n"
      "v_exp_f32 v93, v93\n"
      "s_cbranch_scc1 LSK2_%=\n"
      "global_load_lds_dwordx4 v144, s[40:41] offset:1024 sc1\n"
      "LSK2_%=:\n"
      "s_add_u32 s40, s40, 21504\n"
      "s_addc_u32 s41, s41, 0\n"
      "v_cvt_pk_bf16_f32 v108, v88, v89\n"
      "v_exp_f32 v94, v94\n"
      "v_exp_f32 v95, v95\n"
      "v_cvt_pk_bf16_f32 v109, v90, v91\n"
      "v_cvt_pk_bf16_f32 v110, v92, v93\n"
      "v_cvt_pk_bf16_f32 v111, v94, v95\n"
      "v_mfma_f32_32x32x16_bf16 v[200:215], v[136:139], v[104:107], v[200:215]\n"
      "v_exp_f32 v64, v64\n"
      "v_exp_f32 v65, v65\n"
      "v_exp_f32 v66, v66\n"
      "v_mfma_f32_32x32x16_bf16 v[80:95], v[112:115], v[220:223], 0\n"
      "v_exp_f32 v67, v67\n"
      "v_exp_f32 v68, v68\n"
      "v_exp_f32 v69, v69\n"
      "v_mfma_f32_32x32x16_bf16 v[200:215], v[140:143], v[108:111], v[200:215]\n"
      "v_exp_f32 v70, v70\n"
      "v_exp_f32 v71, v71\n"
      "v_exp_f32 v72, v72\n"
      "v_mfma_f32_32x32x8_bf16 v[80:95], v[116:117], v[234:235], v[80:95]\n"
      "v_exp_f32 v73, v73\n"
      "ds_read_b64 v[120:121], %[vk0] offset:2624\n"
      "ds_read_b64 v[122:123], %[vk0] offset:2880\n"
      "ds_read_b64 v[124:125], %[vkt0] offset:3648\n"
      "ds_read_b128 v[136:139], %[vv0] offset:3904\n"
      "ds_read_b128 v[140:143], %[vv0] offset:4576\n"
      "v_cvt_pk_bf16_f32 v96, v64, v65\n"
      "v_exp_f32 v74, v74\n"
      "v_cvt_pk_bf16_f32 v97, v66, v67\n"
      "v_exp_f32 v75, v75\n"
      "v_cvt_pk_bf16_f32 v98, v68, v69\n"
      "v_exp_f32 v76, v76\n"
      "v_cvt_pk_bf16_f32 v99, v70, v71\n"
      "v_exp_f32 v77, v77\n"
      "v_cvt_pk_bf16_f32 v100, v72, v73\n"
      "v_exp_f32 v78, v78\n"
      "v_exp_f32 v79, v79\n"
      "v_cvt_pk_bf16_f32 v101, v74, v75\n"
      "v_cvt_pk_bf16_f32 v102, v76, v77\n"
      "v_cvt_pk_bf16_f32 v103, v78, v79\n"
      "v_mfma_f32_32x32x16_bf16 v[152:167], v[128:131], v[96:99], v[152:167]\n"
      "v_exp_f32 v80, v80\n"
      "v_exp_f32 v81, v81\n"
      "v_exp_f32 v82, v82\n"
      "v_mfma_f32_32x32x16_bf16 v[64:79], v[112:115], v[224:227], 0\n"
      "v_exp_f32 v83, v83\n"
      "v_exp_f32 v84, v84\n"
      "v_exp_f32 v85, v85\n"
      "v_mfma_f32_32x32x16_bf16 v[152:167], v[132:135], v[100:103], v[152:167]\n"
      "v_exp_f32 v86, v86\n"
      "v_exp_f32 v87, v87\n"
      "v_exp_f32 v88, v88\n"
      "v_mfma_f32_32x32x8_bf16 v[64:79], v[116:117], v[236:237], v[64:79]\n"
      "v_exp_f32 v89, v89\n"
      "v_cvt_pk_bf16_f32 v104, v80, v81\n"
      "v_exp_f32 v90, v90\n"
      "v_cvt_pk_bf16_f32 v105, v82, v83\n"
      "v_exp_f32 v91, v91\n"
      "v_cvt_pk_bf16_f32 v106, v84, v85\n"
      "v_exp_f32 v92, v92\n"
      "v_cvt_pk_bf16_f32 v107, v86, v87\n"
      "v_exp_f32 v93, v93\n"
      "v_cvt_pk_bf16_f32 v108, v88, v89\n"
      "v_exp_f32 v94, v94\n"
      "v_exp_f32 v95, v95\n"
      "v_cvt_pk_bf16_f32 v109, v90, v91\n"
      "v_cvt_pk_bf16_f32 v110, v92, v93\n"
      "v_cvt_pk_bf16_f32 v111, v94, v95\n"
      "v_mfma_f32_32x32x16_bf16 v[168:183], v[128:131], v[104:107], v[168:183]\n"
      "v_exp_f32 v64, v64\n"
      "v_exp_f32 v65, v65\n"
      "v_exp_f32 v66, v66\n"
      "v_mfma_f32_32x32x16_bf16 v[80:95], v[112:115], v[228:231], 0\n"
      "v_exp_f32 v67, v67\n"
      "v_exp_f32 v68, v68\n"
      "v_exp_f32 v69, v69\n"
      "v_mfma_f32_32x32x16_bf16 v[168:183], v[132:135], v[108:111], v[168:183]\n"
      "v_exp_f32 v70, v70\n"
      "v_exp_f32 v71, v71\n"
      "v_exp_f32 v72, v72\n"
      "v_mfma_f32_32x32x8_bf16 v[80:95], v[116:117], v[238:239], v[80:95]\n"
      "v_exp_f32 v73, v73\n"
      "v_cvt_pk_bf16_f32 v96, v64, v65\n"
      "v_exp_f32 v74, v74\n"
      "v_cvt_pk_bf16_f32 v97, v66, v67\n"
      "v_exp_f32 v75, v75\n"
      "v_cvt_pk_bf16_f32 v98, v68, v69\n"
      "v_exp_f32 v76, v76\n"
      "v_cvt_pk_bf16_f32 v99, v70, v71\n"
      "v_exp_f32 v77, v77\n"
      "v_cvt_pk_bf16_f32 v100, v72, v73\n"
      "v_exp_f32 v78, v78\n"
      "v_exp_f32 v79, v79\n"
      "v_cvt_pk_bf16_f32 v101, v74, v75\n"
      "v_cvt_pk_bf16_f32 v102, v76, v77\n"
      "v_cvt_pk_bf16_f32 v103, v78, v79\n"
      "s_waitcnt lgkmcnt(0)\n"
      "v_mfma_f32_32x32x16_bf16 v[184:199], v[128:131], v[96:99], v[184:199]\n"
      "v_exp_f32 v80, v80\n"
      "v_exp_f32 v81, v81\n"
      "v_exp_f32 v82, v82\n"
      "v_mfma_f32_32x32x16_bf16 v[64:79], v[120:123], v[216:219], 0\n"
      "v_exp_f32 v83, v83\n"
      "v_exp_f32 v84, v84\n"
      "v_exp_f32 v85, v85\n"
      "v_mfma_f32_32x32x16_bf16 v[184:199], v[132:135], v[100:103], v[184:199]\n"
      "v_exp_f32 v86, v86\n"
      "v_exp_f32 v87, v87\n"
      "v_exp_f32 v88, v88\n"
      "v_mfma_f32_32x32x8_bf16 v[64:79], v[124:125], v[232:233], v[64:79]\n"
      "v_exp_f32 v89, v89\n"
      "v_cvt_pk_bf16_f32 v104, v80, v81\n"
      "v_exp_f32 v90, v90\n"
      "v_cvt_pk_bf16_f32 v105, v82, v83\n"
      "v_exp_f32 v91, v91\n"
      "v_cvt_pk_bf16_f32 v106, v84, v85\n"
      "v_exp_f32 v92, v92\n"
      "v_cvt_pk_bf16_f32 v107, v86, v87\n"
      "v_exp_f32 v93, v93\n"
      "v_cvt_pk_bf16_f32 v108, v88, v89\n"
      "v_exp_f32 v94, v94\n"
      "v_exp_f32 v95, v95\n"
      "v_cvt_pk_bf16_f32 v109, v90, v91\n"
      "v_cvt_pk_bf16_f32 v110, v92, v93\n"
      "v_cvt_pk_bf16_f32 v111, v94, v95\n"
      "v_mfma_f32_32x32x16_bf16 v[200:215], v[128:131], v[104:107], v[200:215]\n"
      "v_exp_f32 v64, v64\n"
      "v_exp_f32 v65, v65\n"
      "v_exp_f32 v66, v66\n"
      "v_mfma_f32_32x32x16_bf16 v[80:95], v[120:123], v[220:223], 0\n"
      "v_exp_f32 v67, v67\n"
      "v_exp_f32 v68, v68\n"
      "v_exp_f32 v69, v69\n"
      "v_mfma_f32_32x32x16_bf16 v[200:215], v[132:135], v[108:111], v[200:215]\n"
      "v_exp_f32 v70, v70\n"
      "v_exp_f32 v71, v71\n"
      "v_exp_f32 v72, v72\n"
      "v_mfma_f32_32x32x8_bf16 v[80:95], v[124:125], v[234:235], v[80:95]\n"
      "v_exp_f32 v73, v73\n"
      "ds_read_b64 v[112:113], %[vk0] offset:5248\n"
      "ds_read_b64 v[114:115], %[vk0] offset:5504\n"
      "ds_read_b64 v[116:117], %[vkt0] offset:6272\n"
      "ds_read_b128 v[128:131], %[vv0] offset:6528\n"
      "ds_read_b128 v[132:135], %[vv0] offset:7200\n"
      "v_cvt_pk_bf16_f32 v96, v64, v65\n"
      "v_exp_f32 v74, v74\n"
      "v_cvt_pk_bf16_f32 v97, v66, v67\n"
      "v_exp_f32 v75, v75\n"
      "v_cvt_pk_bf16_f32 v98, v68, v69\n"
      "v_exp_f32 v76, v76\n"
      "v_cvt_pk_bf16_f32 v99, v70, v71\n"
      "v_exp_f32 v77, v77\n"
      "v_cvt_pk_bf16_f32 v100, v72, v73\n"
      "v_exp_f32 v78, v78\n"
      "v_exp_f32 v79, v79\n"
      "v_cvt_pk_bf16_f32 v101, v74, v75\n"
      "v_cvt_pk_bf16_f32 v102, v76, v77\n"
      "v_cvt_pk_bf16_f32 v103, v78, v79\n"
      "v_mfma_f32_32x32x16_bf16 v[152:167], v[136:139], v[96:99], v[152:167]\n"
      "v_exp_f32 v80, v80\n"
      "v_exp_f32 v81, v81\n"
      "v_exp_f32 v82, v82\n"
      "v_mfma_f32_32x32x16_bf16 v[64:79], v[120:123], v[224:227], 0\n"
      "v_exp_f32 v83, v83\n"
      "v_exp_f32 v84, v84\n"
      "v_exp_f32 v85, v85\n"
      "v_mfma_f32_32x32x16_bf16 v[152:167], v[140:143], v[100:103], v[152:167]\n"
      "v_exp_f32 v86, v86\n"
      "v_exp_f32 v87, v87\n"
      "v_exp_f32 v88, v88\n"
      "v_mfma_f32_32x32x8_bf16 v[64:79], v[124:125], v[236:237], v[64:79]\n"
      "v_exp_f32 v89, v89\n"
      "v_cvt_pk_bf16_f32 v104, v80, v81\n"
      "v_exp_f32 v90, v90\n"
      "v_cvt_pk_bf16_f32 v105, v82, v83\n"
      "v_exp_f32 v91, v91\n"
      "v_cvt_pk_bf16_f32 v106, v84, v85\n"
      "v_exp_f32 v92, v92\n"
      "v_cvt_pk_bf16_f32 v107, v86, v87\n"
      "v_exp_f32 v93, v93\n"
      "v_cvt_pk_bf16_f32 v108, v88, v89\n"
      "v_exp_f32 v94, v94\n"
      "v_exp_f32 v95, v95\n"
      "v_cvt_pk_bf16_f32 v109, v90, v91\n"
      "v_cvt_pk_bf16_f32 v110, v92, v93\n"
      "v_cvt_pk_bf16_f32 v111, v94, v95\n"
      "v_mfma_f32_32x32x16_bf16 v[168:183], v[136:139], v[104:107], v[168:183]\n"
      "v_exp_f32 v64, v64\n"
      "v_exp_f32 v65, v65\n"
      "v_exp_f32 v66, v66\n"
      "v_mfma_f32_32x32x16_bf16 v[80:95], v[120:123], v[228:231], 0\n"
      "v_exp_f32 v67, v67\n"
      "v_exp_f32 v68, v68\n"
      "v_exp_f32 v69, v69\n"
      "v_mfma_f32_32x32x16_bf16 v[168:183], v[140:143], v[108:111], v[168:183]\n"
      "v_exp_f32 v70, v70\n"
      "v_exp_f32 v71, v71\n"
      "v_exp_f32 v72, v72\n"
      "v_mfma_f32_32x32x8_bf16 v[80:95], v[124:125], v[238:239], v[80:95]\n"
      "v_exp_f32 v73, v73\n"
      "v_cvt_pk_bf16_f32 v96, v64, v65\n"
      "v_exp_f32 v74, v74\n"
      "v_cvt_pk_bf16_f32 v97, v66, v67\n"
      "v_exp_f32 v75, v75\n"
      "v_cvt_pk_bf16_f32 v98, v68, v69\n"
      "v_exp_f32 v76, v76\n"
      "v_cvt_pk_bf16_f32 v99, v70, v71\n"
      "v_exp_f32 v77, v77\n"
      "v_cvt_pk_bf16_f32 v100, v72, v73\n"
      "v_exp_f32 v78, v78\n"
      "v_exp_f32 v79, v79\n"
      "v_cvt_pk_bf16_f32 v101, v74, v75\n"
      "v_cvt_pk_bf16_f32 v102, v76, v77\n"
      "v_cvt_pk_bf16_f32 v103, v78, v79\n"
      "s_waitcnt lgkmcnt(0)\n"
      "v_mfma_f32_32x32x16_bf16 v[184:199], v[136:139], v[96:99], v[184:199]\n"
      "v_exp_f32 v80, v80\n"
      "v_exp_f32 v81, v81\n"
      "v_exp_f32 v82, v82\n"
      "v_mfma_f32_32x32x16_bf16 v[64:79], v[112:115], v[216:219], 0\n"
      "v_exp_f32 v83, v83\n"
      "v_exp_f32 v84, v84\n"
      "v_exp_f32 v85, v85\n"
      "v_mfma_f32_32x32x16_bf16 v[184:199], v[140:143], v[100:103], v[184:199]\n"
      "v_exp_f32 v86, v86\n"
      "v_exp_f32 v87, v87\n"
      "v_exp_f32 v88, v88\n"
      "v_mfma_f32_32x32x8_bf16 v[64:79], v[116:117], v[232:233], v[64:79]\n"
      "v_exp_f32 v89, v89\n"
      "v_cvt_pk_bf16_f32 v104, v80, v81\n"
      "v_exp_f32 v90, v90\n"
      "v_cvt_pk_bf16_f32 v105, v82, v83\n"
      "v_exp_f32 v91, v91\n"
      "v_cvt_pk_bf16_f32 v106, v84, v85\n"
      "v_exp_f32 v92, v92\n"
      "v_cvt_pk_bf16_f32 v107, v86, v87\n"
      "v_exp_f32 v93, v93\n"
      "v_cvt_pk_bf16_f32 v108, v88, v89\n"
      "v_exp_f32 v94, v94\n"
      "v_exp_f32 v95, v95\n"
      "v_cvt_pk_bf16_f32 v109, v90, v91\n"
      "v_cvt_pk_bf16_f32 v110, v92, v93\n"
      "v_cvt_pk_bf16_f32 v111, v94, v95\n"
      "v_mfma_f32_32x32x16_bf16 v[200:215], v[136:139], v[104:107], v[200:215]\n"
      "v_exp_f32 v64, v64\n"
      "v_exp_f32 v65, v65\n"
      "v_exp_f32 v66, v66\n"
      "v_mfma_f32_32x32x16_bf16 v[80:95], v[112:115], v[220:223], 0\n"
      "v_exp_f32 v67, v67\n"
      "v_exp_f32 v68, v68\n"
      "v_exp_f32 v69, v69\n"
      "v_mfma_f32_32x32x16_bf16 v[200:215], v[140:143], v[108:111], v[200:215]\n"
      "v_exp_f32 v70, v70\n"
      "v_exp_f32 v71, v71\n"
      "v_exp_f32 v72, v72\n"
      "v_mfma_f32_32x32x8_bf16 v[80:95], v[116:117], v[234:235], v[80:95]\n"
      "v_exp_f32 v73, v73\n"
      "ds_read_b64 v[120:121], %[vk0] offset:7872\n"
      "ds_read_b64 v[122:123], %[vk0] offset:8128\n"
      "ds_read_b64 v[124:125], %[vkt0] offset:8896\n"
      "ds_read_b128 v[136:139], %[vv0] offset:9152\n"
      "ds_read_b128 v[140:143], %[vv0] offset:9824\n"
      "v_cvt_pk_bf16_f32 v96, v64, v65\n"
      "v_exp_f32 v74, v74\n"
      "v_cvt_pk_bf16_f32 v97, v66, v67\n"
      "v_exp_f32 v75, v75\n"
      "v_cvt_pk_bf16_f32 v98, v68, v69\n"
      "v_exp_f32 v76, v76\n"
      "v_cvt_pk_bf16_f32 v99, v70, v71\n"
      "v_exp_f32 v77, v77\n"
      "v_cvt_pk_bf16_f32 v100, v72, v73\n"
      "v_exp_f32 v78, v78\n"
      "v_exp_f32 v79, v79\n"
      "v_cvt_pk_bf16_f32 v101, v74, v75\n"
      "v_cvt_pk_bf16_f32 v102, v76, v77\n"
      "v_cvt_pk_bf16_f32 v103, v78, v79\n"
      "v_mfma_f32_32x32x16_bf16 v[152:167], v[128:131], v[96:99], v[152:167]\n"
      "v_exp_f32 v80, v80\n"
      "v_exp_f32 v81, v81\n"
      "v_exp_f32 v82, v82\n"
      "v_mfma_f32_32x32x16_bf16 v[64:79], v[112:115], v[224:227], 0\n"
      "v_exp_f32 v83, v83\n"
      "v_exp_f32 v84, v84\n"
      "v_exp_f32 v85, v85\n"
      "v_mfma_f32_32x32x16_bf16 v[152:167], v[132:135], v[100:103], v[152:167]\n"
      "v_exp_f32 v86, v86\n"
      "v_exp_f32 v87, v87\n"
      "v_exp_f32 v88, v88\n"
      "v_mfma_f32_32x32x8_bf16 v[64:79], v[116:117], v[236:237], v[64:79]\n"
      "v_exp_f32 v89, v89\n"
      "v_cvt_pk_bf16_f32 v104, v80, v81\n"
      "v_exp_f32 v90, v90\n"
      "v_cvt_pk_bf16_f32 v105, v82, v83\n"
      "v_exp_f32 v91, v91\n"
      "v_cvt_pk_bf16_f32 v106, v84, v85\n"
      "v_exp_f32 v92, v92\n"
      "v_cvt_pk_bf16_f32 v107, v86, v87\n"
      "v_exp_f32 v93, v93\n"
      "v_cvt_pk_bf16_f32 v108, v88, v89\n"
      "v_exp_f32 v94, v94\n"
      "v_exp_f32 v95, v95\n"
      "v_cvt_pk_bf16_f32 v109, v90, v91\n"
      "v_cvt_pk_bf16_f32 v110, v92, v93\n"
      "v_cvt_pk_bf16_f32 v111, v94, v95\n"
      "v_mfma_f32_32x32x16_bf16 v[168:183], v[128:131], v[104:107], v[168:183]\n"
      "v_exp_f32 v64, v64\n"
      "v_exp_f32 v65, v65\n"
      "v_exp_f32 v66, v66\n"
      "v_mfma_f32_32x32x16_bf16 v[80:95], v[112:115], v[228:231], 0\n"
      "v_exp_f32 v67, v67\n"
      "v_exp_f32 v68, v68\n"
      "v_exp_f32 v69, v69\n"
      "v_mfma_f32_32x32x16_bf16 v[168:183], v[132:135], v[108:111], v[168:183]\n"
      "v_exp_f32 v70, v70\n"
      "v_exp_f32 v71, v71\n"
      "v_exp_f32 v72, v72\n"
      "v_mfma_f32_32x32x8_bf16 v[80:95], v[116:117], v[238:239], v[80:95]\n"
      "v_exp_f32 v73, v73\n"
      "v_add_u32 v145, 1, v145\n"
      "s_waitcnt vmcnt(0) lgkmcnt(0)\n"
      "ds_write_b32 %[vpost], v145\n"
      "v_cvt_pk_bf16_f32 v96, v64, v65\n"
      "v_exp_f32 v74, v74\n"
      "v_cvt_pk_bf16_f32 v97, v66, v67\n"
      "v_exp_f32 v75, v75\n"
      "v_cvt_pk_bf16_f32 v98, v68, v69\n"
      "v_exp_f32 v76, v76\n"
      "v_cvt_pk_bf16_f32 v99, v70, v71\n"
      "v_exp_f32 v77, v77\n"
      "v_cvt_pk_bf16_f32 v100, v72, v73\n"
      "v_exp_f32 v78, v78\n"
      "v_exp_f32 v79, v79\n"
      "v_cvt_pk_bf16_f32 v101, v74, v75\n"
      "v_cvt_pk_bf16_f32 v102, v76, v77\n"
      "v_cvt_pk_bf16_f32 v103, v78, v79\n"
      "v_mfma_f32_32x32x16_bf16 v[184:199], v[128:131], v[96:99], v[184:199]\n"
      "v_exp_f32 v80, v80\n"
      "v_exp_f32 v81, v81\n"
      "v_exp_f32 v82, v82\n"
      "v_mfma_f32_32x32x16_bf16 v[64:79], v[120:123], v[216:219], 0\n"
      "v_exp_f32 v83, v83\n"
      "v_exp_f32 v84, v84\n"
      "v_exp_f32 v85, v85\n"
      "v_mfma_f32_32x32x16_bf16 v[184:199], v[132:135], v[100:103], v[184:199]\n"
      "v_exp_f32 v86, v86\n"
      "v_exp_f32 v87, v87\n"
      "v_exp_f32 v88, v88\n"
      "v_mfma_f32_32x32x8_bf16 v[64:79], v[124:125], v[232:233], v[64:79]\n"
      "v_exp_f32 v89, v89\n"
      "ds_read_b32 v146, %[vchk]\n"
      "v_cvt_pk_bf16_f32 v104, v80, v81\n"
      "v_exp_f32 v90, v90\n"
      "v_cvt_pk_bf16_f32 v105, v82, v83\n"
      "v_exp_f32 v91, v91\n"
      "v_cvt_pk_bf16_f32 v106, v84, v85\n"
      "v_exp_f32 v92, v92\n"
      "v_cvt_pk_bf16_f32 v107, v86, v87\n"
      "v_exp_f32 v93, v93\n"
      "v_cvt_pk_bf16_f32 v108, v88, v89\n"
      "v_exp_f32 v94, v94\n"
      "v_exp_f32 v95, v95\n"
      "s_waitcnt lgkmcnt(0)\n"
      "v_cmp_lt_u32 vcc, v146, v145\n"
      "v_cvt_pk_bf16_f32 v109, v90, v91\n"
      "v_cvt_pk_bf16_f32 v110, v92, v93\n"
      "v_cvt_pk_bf16_f32 v111, v94, v95\n"
      "s_cbranch_vccnz LSPIN3_%=\n"
      "LGO3_%=:\n"
      "v_mfma_f32_32x32x16_bf16 v[200:215], v[128:131], v[104:107], v[200:215]\n"
      "v_exp_f32 v64, v64\n"
      "v_exp_f32 v65, v65\n"
      "v_exp_f32 v66, v66\n"
      "v_mfma_f32_32x32x16_bf16 v[80:95], v[120:123], v[220:223], 0\n"
      "v_exp_f32 v67, v67\n"
      "v_exp_f32 v68, v68\n"
      "v_exp_f32 v69, v69\n"
      "v_mfma_f32_32x32x16_bf16 v[200:215], v[132:135], v[108:111], v[200:215]\n"
      "v_exp_f32 v70, v70\n"
      "v_exp_f32 v71, v71\n"
      "v_exp_f32 v72, v72\n"
      "v_mfma_f32_32x32x8_bf16 v[80:95], v[124:125], v[234:235], v[80:95]\n"
      "v_exp_f32 v73, v73\n"
      "ds_read_b64 v[112:113], %[vk0] offset:21504\n"
      "ds_read_b64 v[114:115], %[vk0] offset:21760\n"
      "ds_read_b64 v[116:117], %[vkt0] offset:22528\n"
      "ds_read_b128 v[128:131], %[vv0] offset:22784\n"
      "ds_read_b128 v[132:135], %[vv0] offset:23456\n"
      "v_cvt_pk_bf16_f32 v96, v64, v65\n"
      "v_exp_f32 v74, v74\n"
      "v_cvt_pk_bf16_f32 v97, v66, v67\n"
      "v_exp_f32 v75, v75\n"
      "v_cvt_pk_bf16_f32 v98, v68, v69\n"
      "v_exp_f32 v76, v76\n"
      "v_cvt_pk_bf16_f32 v99, v70, v71\n"
      "v_exp_f32 v77, v77\n"
      "s_add_u32 m0, %[ldsw], 43008\n"
      "s_cmp_lg_u32 s36, 0\n"
      "v_cvt_pk_bf16_f32 v100, v72, v73\n"
      "v_exp_f32 v78, v78\n"
      "v_exp_f32 v79, v79\n"
      "v_cvt_pk_bf16_f32 v101, v74, v75\n"
      "v_cvt_pk_bf16_f32 v102, v76, v77\n"
      "v_cvt_pk_bf16_f32 v103, v78, v79\n"
      "v_mfma_f32_32x32x16_bf16 v[152:167], v[136:139], v[96:99], v[152:167]\n"
      "v_exp_f32 v80, v80\n"
      "v_exp_f32 v81, v81\n"
      "v_exp_f32 v82, v82\n"
      "v_mfma_f32_32x32x16_bf16 v[64:79], v[120:123], v[224:227], 0\n"
      "v_exp_f32 v83, v83\n"
      "v_exp_f32 v84, v84\n"
      "v_exp_f32 v85, v85\n"
      "v_mfma_f32_32x32x16_bf16 v[152:167], v[140:143], v[100:103], v[152:167]\n"
      "v_exp_f32 v86, v86\n"
      "v_exp_f32 v87, v87\n"
      "v_exp_f32 v88, v88\n"
      "v_mfma_f32_32x32x8_bf16 v[64:79], v[124:125], v[236:237], v[64:79]\n"
      "v_exp_f32 v89, v89\n"
      "s_cbranch_scc1 LSL0_%=\n"
      "global_load_lds_dwordx4 %[voff], s[40:41] offset:0 sc1\n"
      "global_load_lds_dwordx4 %[voff], s[40:41] offset:1024 sc1\n"
      "LSL0_%=:\n"
      "v_cvt_pk_bf16_f32 v104, v80, v81\n"
      "v_exp_f32 v90, v90\n"
      "v_cvt_pk_bf16_f32 v105, v82, v83\n"
      "v_exp_f32 v91, v91\n"
      "v_cvt_pk_bf16_f32 v106, v84, v85\n"
      "v_exp_f32 v92, v92\n"
      "v_cvt_pk_bf16_f32 v107, v86, v87\n"
      "v_exp_f32 v93, v93\n"
      "v_cvt_pk_bf16_f32 v108, v88, v89\n"
      "v_exp_f32 v94, v94\n"
      "v_exp_f32 v95, v95\n"
      "v_cvt_pk_bf16_f32 v109, v90, v91\n"
      "v_cvt_pk_bf16_f32 v110, v92, v93\n"
      "v_cvt_pk_bf16_f32 v111, v94, v95\n"
      "v_mfma_f32_32x32x16_bf16 v[168:183], v[136:139], v[104:107], v[168:183]\n"
      "v_exp_f32 v64, v64\n"
      "v_exp_f32 v65, v65\n"
      "v_exp_f32 v66, v66\n"
      "v_mfma_f32_32x32x16_bf16 v[80:95], v[120:123], v[228:231], 0\n"
      "v_exp_f32 v67, v67\n"
      "v_exp_f32 v68, v68\n"
      "v_exp_f32 v69, v69\n"
      "v_mfma_f32_32x32x16_bf16 v[168:183], v[140:143], v[108:111], v[168:183]\n"
      "v_exp_f32 v70, v70\n"
      "v_exp_f32 v71, v71\n"
      "v_exp_f32 v72, v72\n"
      "v_mfma_f32_32x32x8_bf16 v[80:95], v[124:125], v[238:239], v[80:95]\n"
      "v_exp_f32 v73, v73\n"
      "s_cbranch_scc1 LSL2_%=\n"
      "global_load_lds_dwordx4 %[voff], s[40:41] offset:2048 sc1\n"
      "global_load_lds_dwordx4 %[voff], s[40:41] offset:3072 sc1\n"
      "LSL2_%=:\n"
      "v_cvt_pk_bf16_f32 v96, v64, v65\n"
      "v_exp_f32 v74, v74\n"
      "v_cvt_pk_bf16_f32 v97, v66, v67\n"
      "v_exp_f32 v75, v75\n"
      "v_cvt_pk_bf16_f32 v98, v68, v69\n"
      "v_exp_f32 v76, v76\n"
      "v_cvt_pk_bf16_f32 v99, v70, v71\n"
      "v_exp_f32 v77, v77\n"
      "v_cvt_pk_bf16_f32 v100, v72, v73\n"
      "v_exp_f32 v78, v78\n"
      "v_exp_f32 v79, v79\n"
      "v_cvt_pk_bf16_f32 v101, v74, v75\n"
      "v_cvt_pk_bf16_f32 v102, v76, v77\n"
      "v_cvt_pk_bf16_f32 v103, v78, v79\n"
      "s_waitcnt lgkmcnt(0)\n"
      "v_mfma_f32_32x32x16_bf16 v[184:199], v[136:139], v[96:99], v[184:199]\n"
      "v_exp_f32 v80, v80\n"
      "v_exp_f32 v81, v81\n"
      "v_exp_f32 v82, v82\n"
      "v_mfma_f32_32x32x16_bf16 v[64:79], v[112:115], v[216:219], 0\n"
      "v_exp_f32 v83, v83\n"
      "v_exp_f32 v84, v84\n"
      "v_exp_f32 v85, v85\n"
      "v_mfma_f32_32x32x16_bf16 v[184:199], v[140:143], v[100:103], v[184:199]\n"
      "v_exp_f32 v86, v86\n"
      "v_exp_f32 v87, v87\n"
      "v_exp_f32 v88, v88\n"
      "v_mfma_f32_32x32x8_bf16 v[64:79], v[116:117], v[232:233], v[64:79]\n"
      "v_exp_f32 v89, v89\n"
      "s_cbranch_scc1 LSK3_%=\n"
      "s_add_u32 m0, m0, 0x1000\n"
      "s_cmp_lg_u32 %[w3], 3\n"
      "global_load_lds_dwordx4 v144, s[40:41] offset:0 sc1\n"
      "s_cbranch_scc1 LSK3_%=\n"
      "global_load_lds_dwordx4 v144, s[40:41] offset:1024 sc1\n"
      "LSK3_%=:\n"
      "v_cvt_pk_bf16_f32 v104, v80, v81\n"
      "v_exp_f32 v90, v90\n"
      "v_cvt_pk_bf16_f32 v105, v82, v83\n"
      "v_exp_f32 v91, v91\n"
      "v_cvt_pk_bf16_f32 v106, v84, v85\n"
      "v_exp_f32 v92, v92\n"
      "v_cvt_pk_bf16_f32 v107, v86, v87\n"
      "v_exp_f32 v93, v93\n"
      "s_add_u32 s40, s40, 21504\n"
      "s_addc_u32 s41, s41, 0\n"
      "s_add_u32 s36, s36, 1\n"
      "s_cmp_eq_u32 s36, 2\n"
      "v_cvt_pk_bf16_f32 v108, v88, v89\n"
      "v_exp_f32 v94, v94\n"
      "v_exp_f32 v95, v95\n"
      "v_cvt_pk_bf16_f32 v109, v90, v91\n"
      "v_cvt_pk_bf16_f32 v110, v92, v93\n"
      "v_cvt_pk_bf16_f32 v111, v94, v95\n"
      "s_cbranch_scc0 LOOP_%=\n"
      "v_mfma_f32_32x32x16_bf16 v[200:215], v[136:139], v[104:107], v[200:215]\n"
      "v_exp_f32 v64, v64\n"
      "v_exp_f32 v65, v65\n"
      "v_exp_f32 v66, v66\n"
      "v_mfma_f32_32x32x16_bf16 v[80:95], v[112:115], v[220:223], 0\n"
      "v_exp_f32 v67, v67\n"
      "v_exp_f32 v68, v68\n"
      "v_exp_f32 v69, v69\n"
      "v_mfma_f32_32x32x16_bf16 v[200:215], v[140:143], v[108:111], v[200:215]\n"
      "v_exp_f32 v70, v70\n"
      "v_exp_f32 v71, v71\n"
      "v_exp_f32 v72, v72\n"
      "v_mfma_f32_32x32x8_bf16 v[80:95], v[116:117], v[234:235], v[80:95]\n"
      "v_exp_f32 v73, v73\n"
      "ds_read_b64 v[120:121], %[vk0] offset:24128\n"
      "ds_read_b64 v[122:123], %[vk0] offset:24384\n"
      "ds_read_b64 v[124:125], %[vkt0] offset:25152\n"
      "ds_read_b128 v[136:139], %[vv0] offset:25408\n"
      "ds_read_b128 v[140:143], %[vv0] offset:26080\n"
      "v_cvt_pk_bf16_f32 v96, v64, v65\n"
      "v_exp_f32 v74, v74\n"
      "v_cvt_pk_bf16_f32 v97, v66, v67\n"
      "v_exp_f32 v75, v75\n"
      "v_cvt_pk_bf16_f32 v98, v68, v69\n"
      "v_exp_f32 v76, v76\n"
      "v_cvt_pk_bf16_f32 v99, v70, v71\n"
      "v_exp_f32 v77, v77\n"
      "v_cvt_pk_bf16_f32 v100, v72, v73\n"
      "v_exp_f32 v78, v78\n"
      "v_exp_f32 v79, v79\n"
      "v_cvt_pk_bf16_f32 v101, v74, v75\n"
      "v_cvt_pk_bf16_f32 v102, v76, v77\n"
      "v_cvt_pk_bf16_f32 v103, v78, v79\n"
      "v_mfma_f32_32x32x16_bf16 v[152:167], v[128:131], v[96:99], v[152:167]\n"
      "v_exp_f32 v80, v80\n"
      "v_exp_f32 v81, v81\n"
      "v_exp_f32 v82, v82\n"
      "v_mfma_f32_32x32x16_bf16 v[64:79], v[112:115], v[224:227], 0\n"
      "v_exp_f32 v83, v83\n"
      "v_exp_f32 v84, v84\n"
      "v_exp_f32 v85, v85\n"
      "v_mfma_f32_32x32x16_bf16 v[152:167], v[132:135], v[100:103], v[152:167]\n"
      "v_exp_f32 v86, v86\n"
      "v_exp_f32 v87, v87\n"
      "v_exp_f32 v88, v88\n"
      "v_mfma_f32_32x32x8_bf16 v[64:79], v[116:117], v[236:237], v[64:79]\n"
      "v_exp_f32 v89, v89\n"
      "v_cvt_pk_bf16_f32 v104, v80, v81\n"
      "v_exp_f32 v90, v90\n"
      "v_cvt_pk_bf16_f32 v105, v82, v83\n"
      "v_exp_f32 v91, v91\n"
      "v_cvt_pk_bf16_f32 v106, v84, v85\n"
      "v_exp_f32 v92, v92\n"
      "v_cvt_pk_bf16_f32 v107, v86, v87\n"
      "v_exp_f32 v93, v93\n"
      "v_cvt_pk_bf16_f32 v108, v88, v89\n"
      "v_exp_f32 v94, v94\n"
      "v_exp_f32 v95, v95\n"
      "v_cvt_pk_bf16_f32 v109, v90, v91\n"
      "v_cvt_pk_bf16_f32 v110, v92, v93\n"
      "v_cvt_pk_bf16_f32 v111, v94, v95\n"
      "v_mfma_f32_32x32x16_bf16 v[168:183], v[128:131], v[104:107], v[168:183]\n"
      "v_exp_f32 v64, v64\n"
      "v_exp_f32 v65, v65\n"
      "v_exp_f32 v66, v66\n"
      "v_mfma_f32_32x32x16_bf16 v[80:95], v[112:115], v[228:231], 0\n"
      "v_exp_f32 v67, v67\n"
      "v_exp_f32 v68, v68\n"
      "v_exp_f32 v69, v69\n"
      "v_mfma_f32_32x32x16_bf16 v[168:183], v[132:135], v[108:111], v[168:183]\n"
      "v_exp_f32 v70, v70\n"
      "v_exp_f32 v71, v71\n"
      "v_exp_f32 v72, v72\n"
      "v_mfma_f32_32x32x8_bf16 v[80:95], v[116:117], v[238:239], v[80:95]\n"
      "v_exp_f32 v73, v73\n"
      "v_cvt_pk_bf16_f32 v96, v64, v65\n"
      "v_exp_f32 v74, v74\n"
      "v_cvt_pk_bf16_f32 v97, v66, v67\n"
      "v_exp_f32 v75, v75\n"
      "v_cvt_pk_bf16_f32 v98, v68, v69\n"
      "v_exp_f32 v76, v76\n"
      "v_cvt_pk_bf16_f32 v99, v70, v71\n"
      "v_exp_f32 v77, v77\n"
      "global_load_dwordx2 v[148:149], %[f6], %[ws] sc1\n"
      "v_cvt_pk_bf16_f32 v100, v72, v73\n"
      "v_exp_f32 v78, v78\n"
      "v_exp_f32 v79, v79\n"
      "v_cvt_pk_bf16_f32 v101, v74, v75\n"
      "v_cvt_pk_bf16_f32 v102, v76, v77\n"
      "v_cvt_pk_bf16_f32 v103, v78, v79\n"
      "s_waitcnt lgkmcnt(0)\n"
      "v_mfma_f32_32x32x16_bf16 v[184:199], v[128:131], v[96:99], v[184:199]\n"
      "v_exp_f32 v80, v80\n"
      "v_exp_f32 v81, v81\n"
      "v_exp_f32 v82, v82\n"
      "v_mfma_f32_32x32x16_bf16 v[64:79], v[120:123], v[216:219], 0\n"
      "v_exp_f32 v83, v83\n"
      "v_exp_f32 v84, v84\n"
      "v_exp_f32 v85, v85\n"
      "v_mfma_f32_32x32x16_bf16 v[184:199], v[132:135], v[100:103], v[184:199]\n"
      "v_exp_f32 v86, v86\n"
      "v_exp_f32 v87, v87\n"
      "v_exp_f32 v88, v88\n"
      "v_mfma_f32_32x32x8_bf16 v[64:79], v[124:125], v[232:233], v[64:79]\n"
      "v_exp_f32 v89, v89\n"
      "v_cvt_pk_bf16_f32 v104, v80, v81\n"
      "v_exp_f32 v90, v90\n"
      "v_cvt_pk_bf16_f32 v105, v82, v83\n"
      "v_exp_f32 v91, v91\n"
      "v_cvt_pk_bf16_f32 v106, v84, v85\n"
      "v_exp_f32 v92, v92\n"
      "v_cvt_pk_bf16_f32 v107, v86, v87\n"
      "v_exp_f32 v93, v93\n"
      "v_cvt_pk_bf16_f32 v108, v88, v89\n"
      "v_exp_f32 v94, v94\n"
      "v_exp_f32 v95, v95\n"
      "v_cvt_pk_bf16_f32 v109, v90, v91\n"
      "v_cvt_pk_bf16_f32 v110, v92, v93\n"
      "v_cvt_pk_bf16_f32 v111, v94, v95\n"
      "v_mfma_f32_32x32x16_bf16 v[200:215], v[128:131], v[104:107], v[200:215]\n"
      "v_exp_f32 v64, v64\n"
      "v_exp_f32 v65, v65\n"
      "v_exp_f32 v66, v66\n"
      "v_mfma_f32_32x32x16_bf16 v[80:95], v[120:123], v[220:223], 0\n"
      "v_exp_f32 v67, v67\n"
      "v_exp_f32 v68, v68\n"
      "v_exp_f32 v69, v69\n"
      "v_mfma_f32_32x32x16_bf16 v[200:215], v[132:135], v[108:111], v[200:215]\n"
      "v_exp_f32 v70, v70\n"
      "v_exp_f32 v71, v71\n"
      "v_exp_f32 v72, v72\n"
      "v_mfma_f32_32x32x8_bf16 v[80:95], v[124:125], v[234:235], v[80:95]\n"
      "v_exp_f32 v73, v73\n"
      "ds_read_b64 v[112:113], %[vk0] offset:26752\n"
      "ds_read_b64 v[114:115], %[vk0] offset:27008\n"
      "ds_read_b64 v[116:117], %[vkt0] offset:27776\n"
      "ds_read_b128 v[128:131], %[vv0] offset:28032\n"
      "ds_read_b128 v[132:135], %[vv0] offset:28704\n"
      "v_cvt_pk_bf16_f32 v96, v64, v65\n"
      "v_exp_f32 v74, v74\n"
      "v_cvt_pk_bf16_f32 v97, v66, v67\n"
      "v_exp_f32 v75, v75\n"
      "v_cvt_pk_bf16_f32 v98, v68, v69\n"
      "v_exp_f32 v76, v76\n"
      "v_cvt_pk_bf16_f32 v99, v70, v71\n"
      "v_exp_f32 v77, v77\n"
      "v_cvt_pk_bf16_f32 v100, v72, v73\n"
      "v_exp_f32 v78, v78\n"
      "v_exp_f32 v79, v79\n"
      "v_cvt_pk_bf16_f32 v101, v74, v75\n"
      "v_cvt_pk_bf16_f32 v102, v76, v77\n"
      "v_cvt_pk_bf16_f32 v103, v78, v79\n"
      "v_mfma_f32_32x32x16_bf16 v[152:167], v[136:139], v[96:99], v[152:167]\n"
      "v_exp_f32 v80, v80\n"
      "v_exp_f32 v81, v81\n"
      "v_exp_f32 v82, v82\n"
      "v_mfma_f32_32x32x16_bf16 v[64:79], v[120:123], v[224:227], 0\n"
      "v_exp_f32 v83, v83\n"
      "v_exp_f32 v84, v84\n"
      "v_exp_f32 v85, v85\n"
      "v_mfma_f32_32x32x16_bf16 v[152:167], v[140:143], v[100:103], v[152:167]\n"
      "v_exp_f32 v86, v86\n"
      "v_exp_f32 v87, v87\n"
      "v_exp_f32 v88, v88\n"
      "v_mfma_f32_32x32x8_bf16 v[64:79], v[124:125], v[236:237], v[64:79]\n"
      "v_exp_f32 v89, v89\n"
      "v_cvt_pk_bf16_f32 v104, v80, v81\n"
      "v_exp_f32 v90, v90\n"
      "v_cvt_pk_bf16_f32 v105, v82, v83\n"
      "v_exp_f32 v91, v91\n"
      "v_cvt_pk_bf16_f32 v106, v84, v85\n"
      "v_exp_f32 v92, v92\n"
      "v_cvt_pk_bf16_f32 v107, v86, v87\n"
      "v_exp_f32 v93, v93\n"
      "v_cvt_pk_bf16_f32 v108, v88, v89\n"
      "v_exp_f32 v94, v94\n"
      "v_exp_f32 v95, v95\n"
      "v_cvt_pk_bf16_f32 v109, v90, v91\n"
      "v_cvt_pk_bf16_f32 v110, v92, v93\n"
      "v_cvt_pk_bf16_f32 v111, v94, v95\n"
      "v_mfma_f32_32x32x16_bf16 v[168:183], v[136:139], v[104:107], v[168:183]\n"
      "v_exp_f32 v64, v64\n"
      "v_exp_f32 v65, v65\n"
      "v_exp_f32 v66, v66\n"
      "v_mfma_f32_32x32x16_bf16 v[80:95], v[120:123], v[228:231], 0\n"
      "v_exp_f32 v67, v67\n"
      "v_exp_f32 v68, v68\n"
      "v_exp_f32 v69, v69\n"
      "v_mfma_f32_32x32x16_bf16 v[168:183], v[140:143], v[108:111], v[168:183]\n"
      "v_exp_f32 v70, v70\n"
      "v_exp_f32 v71, v71\n"
      "v_exp_f32 v72, v72\n"
      "v_mfma_f32_32x32x8_bf16 v[80:95], v[124:125], v[238:239], v[80:95]\n"
      "v_exp_f32 v73, v73\n"
      "v_cvt_pk_bf16_f32 v96, v64, v65\n"
      "v_exp_f32 v74, v74\n"
      "v_cvt_pk_bf16_f32 v97, v66, v67\n"
      "v_exp_f32 v75, v75\n"
      "v_cvt_pk_bf16_f32 v98, v68, v69\n"
      "v_exp_f32 v76, v76\n"
      "v_cvt_pk_bf16_f32 v99, v70, v71\n"
      "v_exp_f32 v77, v77\n"
      "v_cvt_pk_bf16_f32 v100, v72, v73\n"
      "v_exp_f32 v78, v78\n"
      "v_exp_f32 v79, v79\n"
      "v_cvt_pk_bf16_f32 v101, v74, v75\n"
      "v_cvt_pk_bf16_f32 v102, v76, v77\n"
      "v_cvt_pk_bf16_f32 v103, v78, v79\n"
      "s_waitcnt lgkmcnt(0)\n"
      "v_mfma_f32_32x32x16_bf16 v[184:199], v[136:139], v[96:99], v[184:199]\n"
      "v_exp_f32 v80, v80\n"
      "v_exp_f32 v81, v81\n"
      "v_exp_f32 v82, v82\n"
      "v_mfma_f32_32x32x16_bf16 v[64:79], v[112:115], v[216:219], 0\n"
      "v_exp_f32 v83, v83\n"
      "v_exp_f32 v84, v84\n"
      "v_exp_f32 v85, v85\n"
      "v_mfma_f32_32x32x16_bf16 v[184:199], v[140:143], v[100:103], v[184:199]\n"
      "v_exp_f32 v86, v86\n"
      "v_exp_f32 v87, v87\n"
      "v_exp_f32 v88, v88\n"
      "v_mfma_f32_32x32x8_bf16 v[64:79], v[116:117], v[232:233], v[64:79]\n"
      "v_exp_f32 v89, v89\n"
      "v_cvt_pk_bf16_f32 v104, v80, v81\n"
      "v_exp_f32 v90, v90\n"
      "v_cvt_pk_bf16_f32 v105, v82, v83\n"
      "v_exp_f32 v91, v91\n"
      "v_cvt_pk_bf16_f32 v106, v84, v85\n"
      "v_exp_f32 v92, v92\n"
      "v_cvt_pk_bf16_f32 v107, v86, v87\n"
      "v_exp_f32 v93, v93\n"
      "v_cvt_pk_bf16_f32 v108, v88, v89\n"
      "v_exp_f32 v94, v94\n"
      "v_exp_f32 v95, v95\n"
      "v_cvt_pk_bf16_f32 v109, v90, v91\n"
      "v_cvt_pk_bf16_f32 v110, v92, v93\n"
      "v_cvt_pk_bf16_f32 v111, v94, v95\n"
      "v_mfma_f32_32x32x16_bf16 v[200:215], v[136:139], v[104:107], v[200:215]\n"
      "v_exp_f32 v64, v64\n"
      "v_exp_f32 v65, v65\n"
      "v_exp_f32 v66, v66\n"
      "v_mfma_f32_32x32x16_bf16 v[80:95], v[112:115], v[220:223], 0\n"
      "v_exp_f32 v67, v67\n"
      "v_exp_f32 v68, v68\n"
      "v_exp_f32 v69, v69\n"
      "v_mfma_f32_32x32x16_bf16 v[200:215], v[140:143], v[108:111], v[200:215]\n"
      "v_exp_f32 v70, v70\n"
      "v_exp_f32 v71, v71\n"
      "v_exp_f32 v72, v72\n"
      "v_mfma_f32_32x32x8_bf16 v[80:95], v[116:117], v[234:235], v[80:95]\n"
      "v_exp_f32 v73, v73\n"
      "ds_read_b64 v[120:121], %[vk0] offset:29376\n"
      "ds_read_b64 v[122:123], %[vk0] offset:29632\n"
      "ds_read_b64 v[124:125], %[vkt0] offset:30400\n"
      "ds_read_b128 v[136:139], %[vv0] offset:30656\n"
      "ds_read_b128 v[140:143], %[vv0] offset:31328\n"
      "v_cvt_pk_bf16_f32 v96, v64, v65\n"
      "v_exp_f32 v74, v74\n"
      "v_cvt_pk_bf16_f32 v97, v66, v67\n"
      "v_exp_f32 v75, v75\n"
      "v_cvt_pk_bf16_f32 v98, v68, v69\n"
      "v_exp_f32 v76, v76\n"
      "v_cvt_pk_bf16_f32 v99, v70, v71\n"
      "v_exp_f32 v77, v77\n"
      "v_cvt_pk_bf16_f32 v100, v72, v73\n"
      "v_exp_f32 v78, v78\n"
      "v_exp_f32 v79, v79\n"
      "v_cvt_pk_bf16_f32 v101, v74, v75\n"
      "v_cvt_pk_bf16_f32 v102, v76, v77\n"
      "v_cvt_pk_bf16_f32 v103, v78, v79\n"
      "v_mfma_f32_32x32x16_bf16 v[152:167], v[128:131], v[96:99], v[152:167]\n"
      "v_exp_f32 v80, v80\n"
      "v_exp_f32 v81, v81\n"
      "v_exp_f32 v82, v82\n"
      "v_mfma_f32_32x32x16_bf16 v[64:79], v[112:115], v[224:227], 0\n"
      "v_exp_f32 v83, v83\n"
      "v_exp_f32 v84, v84\n"
      "v_exp_f32 v85, v85\n"
      "v_mfma_f32_32x32x16_bf16 v[152:167], v[132:135], v[100:103], v[152:167]\n"
      "v_exp_f32 v86, v86\n"
      "v_exp_f32 v87, v87\n"
      "v_exp_f32 v88, v88\n"
      "v_mfma_f32_32x32x8_bf16 v[64:79], v[116:117], v[236:237], v[64:79]\n"
      "v_exp_f32 v89, v89\n"
      "v_cvt_pk_bf16_f32 v104, v80, v81\n"
      "v_exp_f32 v90, v90\n"
      "v_cvt_pk_bf16_f32 v105, v82, v83\n"
      "v_exp_f32 v91, v91\n"
      "v_cvt_pk_bf16_f32 v106, v84, v85\n"
      "v_exp_f32 v92, v92\n"
      "v_cvt_pk_bf16_f32 v107, v86, v87\n"
      "v_exp_f32 v93, v93\n"
      "v_cvt_pk_bf16_f32 v108, v88, v89\n"
      "v_exp_f32 v94, v94\n"
      "v_exp_f32 v95, v95\n"
      "v_cvt_pk_bf16_f32 v109, v90, v91\n"
      "v_cvt_pk_bf16_f32 v110, v92, v93\n"
      "v_cvt_pk_bf16_f32 v111, v94, v95\n"
      "v_mfma_f32_32x32x16_bf16 v[168:183], v[128:131], v[104:107], v[168:183]\n"
      "v_exp_f32 v64, v64\n"
      "v_exp_f32 v65, v65\n"
      "v_exp_f32 v66, v66\n"
      "v_mfma_f32_32x32x16_bf16 v[80:95], v[112:115], v[228:231], 0\n"
      "v_exp_f32 v67, v67\n"
      "v_exp_f32 v68, v68\n"
      "v_exp_f32 v69, v69\n"
      "v_mfma_f32_32x32x16_bf16 v[168:183], v[132:135], v[108:111], v[168:183]\n"
      "v_exp_f32 v70, v70\n"
      "v_exp_f32 v71, v71\n"
      "v_exp_f32 v72, v72\n"
      "v_mfma_f32_32x32x8_bf16 v[80:95], v[116:117], v[238:239], v[80:95]\n"
      "v_exp_f32 v73, v73\n"
      "v_cvt_pk_bf16_f32 v96, v64, v65\n"
      "v_exp_f32 v74, v74\n"
      "v_cvt_pk_bf16_f32 v97, v66, v67\n"
      "v_exp_f32 v75, v75\n"
      "v_cvt_pk_bf16_f32 v98, v68, v69\n"
      "v_exp_f32 v76, v76\n"
      "v_cvt_pk_bf16_f32 v99, v70, v71\n"
      "v_exp_f32 v77, v77\n"
      "v_cvt_pk_bf16_f32 v100, v72, v73\n"
      "v_exp_f32 v78, v78\n"
      "v_exp_f32 v79, v79\n"
      "s_waitcnt vmcnt(0)\n"
      "v_cmp_ne_u64 vcc, %[tag], v[148:149]\n"
      "v_cvt_pk_bf16_f32 v101, v74, v75\n"
      "v_cvt_pk_bf16_f32 v102, v76, v77\n"
      "v_cvt_pk_bf16_f32 v103, v78, v79\n"
      "s_cbranch_vccnz LFLAG6_%=\n"
      "LFLAGGO6_%=:\n"
      "s_waitcnt lgkmcnt(0)\n"
      "v_mfma_f32_32x32x16_bf16 v[184:199], v[128:131], v[96:99], v[184:199]\n"
      "v_exp_f32 v80, v80\n"
      "v_exp_f32 v81, v81\n"
      "v_exp_f32 v82, v82\n"
      "v_mfma_f32_32x32x16_bf16 v[64:79], v[120:123], v[216:219], 0\n"
      "v_exp_f32 v83, v83\n"
      "v_exp_f32 v84, v84\n"
      "v_exp_f32 v85, v85\n"
      "v_mfma_f32_32x32x16_bf16 v[184:199], v[132:135], v[100:103], v[184:199]\n"
      "v_exp_f32 v86, v86\n"
      "v_exp_f32 v87, v87\n"
      "v_exp_f32 v88, v88\n"
      "v_mfma_f32_32x32x8_bf16 v[64:79], v[124:125], v[232:233], v[64:79]\n"
      "v_exp_f32 v89, v89\n"
      "v_cvt_pk_bf16_f32 v104, v80, v81\n"
      "v_exp_f32 v90, v90\n"
      "v_cvt_pk_bf16_f32 v105, v82, v83\n"
      "v_exp_f32 v91, v91\n"
      "v_cvt_pk_bf16_f32 v106, v84, v85\n"
      "v_exp_f32 v92, v92\n"
      "v_cvt_pk_bf16_f32 v107, v86, v87\n"
      "v_exp_f32 v93, v93\n"
      "v_cvt_pk_bf16_f32 v108, v88, v89\n"
      "v_exp_f32 v94, v94\n"
      "v_exp_f32 v95, v95\n"
      "v_cvt_pk_bf16_f32 v109, v90, v91\n"
      "v_cvt_pk_bf16_f32 v110, v92, v93\n"
      "v_cvt_pk_bf16_f32 v111, v94, v95\n"
      "v_mfma_f32_32x32x16_bf16 v[200:215], v[128:131], v[104:107], v[200:215]\n"
      "v_exp_f32 v64, v64\n"
      "v_exp_f32 v65, v65\n"
      "v_exp_f32 v66, v66\n"
      "v_mfma_f32_32x32x16_bf16 v[80:95], v[120:123], v[220:223], 0\n"
      "v_exp_f32 v67, v67\n"
      "v_exp_f32 v68, v68\n"
      "v_exp_f32 v69, v69\n"
      "v_mfma_f32_32x32x16_bf16 v[200:215], v[132:135], v[108:111], v[200:215]\n"
      "v_exp_f32 v70, v70\n"
      "v_exp_f32 v71, v71\n"
      "v_exp_f32 v72, v72\n"
      "v_mfma_f32_32x32x8_bf16 v[80:95], v[124:125], v[234:235], v[80:95]\n"
      "v_exp_f32 v73, v73\n"
      "v_cvt_pk_bf16_f32 v96, v64, v65\n"
      "v_exp_f32 v74, v74\n"
      "v_cvt_pk_bf16_f32 v97, v66, v67\n"
      "v_exp_f32 v75, v75\n"
      "v_cvt_pk_bf16_f32 v98, v68, v69\n"
      "v_exp_f32 v76, v76\n"
      "v_cvt_pk_bf16_f32 v99, v70, v71\n"
      "v_exp_f32 v77, v77\n"
      "v_cvt_pk_bf16_f32 v100, v72, v73\n"
      "v_exp_f32 v78, v78\n"
      "v_exp_f32 v79, v79\n"
      "v_cvt_pk_bf16_f32 v101, v74, v75\n"
      "v_cvt_pk_bf16_f32 v102, v76, v77\n"
      "v_cvt_pk_bf16_f32 v103, v78, v79\n"
      "v_mfma_f32_32x32x16_bf16 v[152:167], v[136:139], v[96:99], v[152:167]\n"
      "v_exp_f32 v80, v80\n"
      "v_exp_f32 v81, v81\n"
      "v_exp_f32 v82, v82\n"
      "v_mfma_f32_32x32x16_bf16 v[64:79], v[120:123], v[224:227], 0\n"
      "v_exp_f32 v83, v83\n"
      "v_exp_f32 v84, v84\n"
      "v_exp_f32 v85, v85\n"
      "v_mfma_f32_32x32x16_bf16 v[152:167], v[140:143], v[100:103], v[152:167]\n"
      "v_exp_f32 v86, v86\n"
      "v_exp_f32 v87, v87\n"
      "v_exp_f32 v88, v88\n"
      "v_mfma_f32_32x32x8_bf16 v[64:79], v[124:125], v[236:237], v[64:79]\n"
      "v_exp_f32 v89, v89\n"
      "v_cvt_pk_bf16_f32 v104, v80, v81\n"
      "v_exp_f32 v90, v90\n"
      "v_cvt_pk_bf16_f32 v105, v82, v83\n"
      "v_exp_f32 v91, v91\n"
      "v_cvt_pk_bf16_f32 v106, v84, v85\n"
      "v_exp_f32 v92, v92\n"
      "v_cvt_pk_bf16_f32 v107, v86, v87\n"
      "v_exp_f32 v93, v93\n"
      "v_cvt_pk_bf16_f32 v108, v88, v89\n"
      "v_exp_f32 v94, v94\n"
      "v_exp_f32 v95, v95\n"
      "v_cvt_pk_bf16_f32 v109, v90, v91\n"
      "v_cvt_pk_bf16_f32 v110, v92, v93\n"
      "v_cvt_pk_bf16_f32 v111, v94, v95\n"
      "v_mfma_f32_32x32x16_bf16 v[168:183], v[136:139], v[104:107], v[168:183]\n"
      "v_exp_f32 v64, v64\n"
      "v_exp_f32 v65, v65\n"
      "v_exp_f32 v66, v66\n"
      "v_mfma_f32_32x32x16_bf16 v[80:95], v[120:123], v[228:231], 0\n"
      "v_exp_f32 v67, v67\n"
      "v_exp_f32 v68, v68\n"
      "v_exp_f32 v69, v69\n"
      "v_mfma_f32_32x32x16_bf16 v[168:183], v[140:143], v[108:111], v[168:183]\n"
      "v_exp_f32 v70, v70\n"
      "v_exp_f32 v71, v71\n"
      "v_exp_f32 v72, v72\n"
      "v_mfma_f32_32x32x8_bf16 v[80:95], v[124:125], v[238:239], v[80:95]\n"
      "v_exp_f32 v73, v73\n"
      "v_cvt_pk_bf16_f32 v96, v64, v65\n"
      "v_exp_f32 v74, v74\n"
      "v_cvt_pk_bf16_f32 v97, v66, v67\n"
      "v_exp_f32 v75, v75\n"
      "v_cvt_pk_bf16_f32 v98, v68, v69\n"
      "v_exp_f32 v76, v76\n"
      "v_cvt_pk_bf16_f32 v99, v70, v71\n"
      "v_exp_f32 v77, v77\n"
      "ds_write_b128 %[vxw], v[152:155] offset:0\n"
      "ds_write_b128 %[vxw], v[156:159] offset:1024\n"
      "ds_write_b128 %[vxw], v[160:163] offset:2048\n"
      "v_cvt_pk_bf16_f32 v100, v72, v73\n"
      "v_exp_f32 v78, v78\n"
      "v_exp_f32 v79, v79\n"
      "v_cvt_pk_bf16_f32 v101, v74, v75\n"
      "v_cvt_pk_bf16_f32 v102, v76, v77\n"
      "v_cvt_pk_bf16_f32 v103, v78, v79\n"
      "s_waitcnt lgkmcnt(0)\n"
      "v_mfma_f32_32x32x16_bf16 v[184:199], v[136:139], v[96:99], v[184:199]\n"
      "v_exp_f32 v80, v80\n"
      "v_exp_f32 v81, v81\n"
      "v_exp_f32 v82, v82\n"
      "v_exp_f32 v83, v83\n"
      "v_exp_f32 v84, v84\n"
      "v_exp_f32 v85, v85\n"
      "v_mfma_f32_32x32x16_bf16 v[184:199], v[140:143], v[100:103], v[184:199]\n"
      "v_exp_f32 v86, v86\n"
      "v_exp_f32 v87, v87\n"
      "v_exp_f32 v88, v88\n"
      "v_exp_f32 v89, v89\n"
      "v_cvt_pk_bf16_f32 v104, v80, v81\n"
      "v_exp_f32 v90, v90\n"
      "v_cvt_pk_bf16_f32 v105, v82, v83\n"
      "v_exp_f32 v91, v91\n"
      "v_cvt_pk_bf16_f32 v106, v84, v85\n"
      "v_exp_f32 v92, v92\n"
      "v_cvt_pk_bf16_f32 v107, v86, v87\n"
      "v_exp_f32 v93, v93\n"
      "ds_write_b128 %[vxw], v[168:171] offset:3072\n"
      "ds_write_b128 %[vxw], v[172:175] offset:4096\n"
      "ds_write_b128 %[vxw], v[176:179] offset:5120\n"
      "v_cvt_pk_bf16_f32 v108, v88, v89\n"
      "v_exp_f32 v94, v94\n"
      "v_exp_f32 v95, v95\n"
      "v_cvt_pk_bf16_f32 v109, v90, v91\n"
      "v_cvt_pk_bf16_f32 v110, v92, v93\n"
      "v_cvt_pk_bf16_f32 v111, v94, v95\n"
      "v_mfma_f32_32x32x16_bf16 v[200:215], v[136:139], v[104:107], v[200:215]\n"
      "s_waitcnt lgkmcnt(0)\n"
      "s_barrier\n"
      "v_mfma_f32_32x32x16_bf16 v[200:215], v[140:143], v[108:111], v[200:215]\n"
      "ds_read_b128 v[64:67], %[vxr] offset:0\n"
      "ds_read_b128 v[68:71], %[vxr] offset:1024\n"
      "ds_read_b128 v[72:75], %[vxr] offset:2048\n"
      "ds_read_b128 v[76:79], %[vxr] offset:3072\n"
      "ds_read_b128 v[80:83], %[vxr] offset:4096\n"
      "ds_read_b128 v[84:87], %[vxr] offset:5120\n"
      "s_branch LEND_%=\n"
      "LFLAG0_%=:\n"
      "s_mov_b32 s44, 0x10000\n"
      "LFLAGB0_%=:\n"
      "s_sleep 2\n"
      "global_load_dwordx2 v[148:149], %[f0], %[ws] sc1\n"
      "s_sub_u32 s44, s44, 1\n"
      "s_cmp_eq_u32 s44, 0\n"
      "s_cbranch_scc1 LFLAGGO0_%=\n"
      "s_waitcnt vmcnt(0)\n"
      "v_cmp_ne_u64 vcc, %[tag], v[148:149]\n"
      "s_nop 4\n"
      "s_cbranch_vccnz LFLAGB0_%=\n"
      "s_branch LFLAGGO0_%=\n"
      "LSPIN0_%=:\n"
      "ds_read_b32 v146, %[vchk]\n"
      "s_waitcnt lgkmcnt(0)\n"
      "v_cmp_lt_u32 vcc, v146, v145\n"
      "s_nop 4\n"
      "s_cbranch_vccnz LSPIN0_%=\n"
      "s_branch LGO0_%=\n"
      "LSPIN1_%=:\n"
      "ds_read_b32 v146, %[vchk]\n"
      "s_waitcnt lgkmcnt(0)\n"
      "v_cmp_lt_u32 vcc, v146, v145\n"
      "s_nop 4\n"
      "s_cbranch_vccnz LSPIN1_%=\n"
      "s_branch LGO1_%=\n"
      "LSPIN2_%=:\n"
      "ds_read_b32 v146, %[vchk]\n"
      "s_waitcnt lgkmcnt(0)\n"
      "v_cmp_lt_u32 vcc, v146, v145\n"
      "s_nop 4\n"
      "s_cbranch_vccnz LSPIN2_%=\n"
      "s_branch LGO2_%=\n"
      "LSPIN3_%=:\n"
      "ds_read_b32 v146, %[vchk]\n"
      "s_waitcnt lgkmcnt(0)\n"
      "v_cmp_lt_u32 vcc, v146, v145\n"
      "s_nop 4\n"
      "s_cbranch_vccnz LSPIN3_%=\n"
      "s_branch LGO3_%=\n"
      "LFLAG6_%=:\n"
      "s_mov_b32 s44, 0x10000\n"
      "LFLAGB6_%=:\n"
      "s_sleep 2\n"
      "global_load_dwordx2 v[148:149], %[f6], %[ws] sc1\n"
      "s_sub_u32 s44, s44, 1\n"
      "s_cmp_eq_u32 s44, 0\n"
      "s_cbranch_scc1 LFLAGGO6_%=\n"
      "s_waitcnt vmcnt(0)\n"
      "v_cmp_ne_u64 vcc, %[tag], v[148:149]\n"
      "s_nop 4\n"
      "s_cbranch_vccnz LFLAGB6_%=\n"
      "s_branch LFLAGGO6_%=\n"
      "LEND_%=:\n"
      "s_waitcnt lgkmcnt(3)\n"
      "v_pk_add_f32 v[184:185], v[184:185], v[64:65]\n"
      "v_pk_add_f32 v[186:187], v[186:187], v[66:67]\n"
      "v_pk_add_f32 v[188:189], v[188:189], v[68:69]\n"
      "v_pk_add_f32 v[190:191], v[190:191], v[70:71]\n"
      "v_pk_add_f32 v[192:193], v[192:193], v[72:73]\n"
      "v_pk_add_f32 v[194:195], v[194:195], v[74:75]\n"
      "s_waitcnt lgkmcnt(0)\n"
      "v_pk_add_f32 v[200:201], v[200:201], v[76:77]\n"
      "v_pk_add_f32 v[202:203], v[202:203], v[78:79]\n"
      "v_pk_add_f32 v[204:205], v[204:205], v[80:81]\n"
      "v_pk_add_f32 v[206:207], v[206:207], v[82:83]\n"
      "v_pk_add_f32 v[208:209], v[208:209], v[84:85]\n"
      "v_pk_add_f32 v[210:211], v[210:211], v[86:87]\n"
      : "=&{v[184:199]}"(o0), "=&{v[200:215]}"(o1) ADBG_OP
      : [vq0] "v"(op[0]), [vq1] "v"(op[1]), [vk0] "v"(op[2]), [vkt0] "v"(op[3]), [vv0] "v"(op[4]), [vm0] "v"(op[5]), [vmt0] "v"(op[6]), [vxw] "v"(op[7]), [vxr] "v"(op[8]),
        [vpost] "v"(op[9]), [vchk] "v"(op[10]), [f6] "v"(op[11]), [voff] "v"(voff), [f0] "v"(fo),
        [ws] "s"(ws), [tag] "s"(tag), [gb] "s"(kv + 2 * KV_CHUNK), [ldsw] "s"(l0 + w * 5120), [w3] "s"(w)
      : "v64", "v65", "v66", "v67", "v68", "v69", "v70", "v71", "v72", "v73", "v74", "v75", "v76", "v77", "v78", "v79", "v80", "v81", "v82", "v83", "v84", "v85", "v86", "v87", "v88", "v89", "v90", "v91", "v92", "v93", "v94", "v95", "v96", "v97", "v98", "v99", "v100", "v101", "v102", "v103", "v104", "v105", "v106", "v107", "v108", "v109", "v110", "v111", "v112", "v113", "v114", "v115", "v116", "v117", "v118", "v119", "v120", "v121", "v122", "v123", "v124", "v125", "v126", "v127", "v128", "v129", "v130", "v131", "v132", "v133", "v134", "v135", "v136", "v137", "v138", "v139", "v140", "v141", "v142", "v143", "v144", "v145", "v146", "v147", "v148", "v149", "v150", "v151", "v152", "v153", "v154", "v155", "v156", "v157", "v158", "v159", "v160", "v161", "v162", "v163", "v164", "v165", "v166", "v167", "v168", "v169", "v170", "v171", "v172", "v173", "v174", "v175", "v176", "v177", "v178", "v179", "v180", "v181", "v182", "v183", "v216", "v217", "v218", "v219", "v220", "v221", "v222", "v223", "v224", "v225", "v226", "v227", "v228", "v229", "v230", "v231", "v232", "v233", "v234", "v235", "v236", "v237", "v238", "v239", "s36", "s40", "s41", "s44", "scc", "memory", "vcc"
  );
  STAMP(ws, 0, 6);
  float l0_ = row_sum(o0), l1_ = row_sum(o1), amax = 0.f;
#pragma unroll
  for (int i = 0; i < 12; ++i) amax = __builtin_fmaxf(__builtin_fmaxf(amax, __builtin_fabsf(o0[i])), __builtin_fabsf(o1[i]));
  if (__builtin_expect(!__all((l0_ >= 0.25f) & (l0_ <= 4294967296.f) & (l1_ >= 0.25f) & (l1_ <= 4294967296.f) & (amax < 1e37f)), 0)) {
    attn_slow(o0, rs, qo + 2 * kh * 1280, WS_KV + head * KV_HEAD, lane);
    attn_slow(o1, rs, qo + (2 * kh + 1) * 1280, WS_KV + head * KV_HEAD, lane);
    l0_ = row_sum(o0); l1_ = row_sum(o1);
  }
  attn_store(o0, l0_, rs, odd, odd && !h, vab, vc, sa, sb, sc);
  attn_store(o1, l1_, rs, odd, odd && !h, vab + 512, vc + 512, sa, sb, sc);
}

struct WoRegs { f32x16 q[3]; f32x8 q3; f32x4 q4; };
#define WQ_OPS(c) c "{v[116:131]}"(wq.q[0]), c "{v[132:147]}"(wq.q[1]), c "{v[148:163]}"(wq.q[2]), c "{v[164:171]}"(wq.q3), c "{v[172:175]}"(wq.q4)
DI void gateup_tile(const u16* w1, const u16* w3, char* ws, LAS char* sm, int rt, int ct, int k0, const char* wo_img, WoRegs& wq, int w, u64 tag, u64 t0, unsigned gate) {
  int lane = fresh_lane();
  const int wr = w >> 1, wc = w & 1;
  const unsigned l0 = lds_addr(sm);
  const rsrc_t rs = ws_rsrc(ws);
  while (__builtin_amdgcn_s_memrealtime() - t0 < TLN) __builtin_amdgcn_s_sleep(4);
  await(ws, WS_LNF + (rt * 16 + (lane & 15)) * 8, ~0u, tag, w);
  unsigned wv[3], wa[3];
#pragma unroll
  for (int i = 0; i < 3; ++i) { const unsigned p = (wc * 3 + i) * 64 + lane, row = p / 12u, pos = p - row * 12u; wv[i] = (row * (F_ * 2) + pos * 16) ^ (wr * 32); }
  const int q = (lane & 15) >> 2, pp = lane & 3, blk = (lane >> 4) & 1;
#pragma unroll
  for (int j = 0; j < 3; ++j) wa[j] = l0 + 32768 + blk * 24576 + (8 * (lane >> 5) + q) * 192 + (((3 * wc + j) ^ blk) * 32) + pp * 8;
  f32x16 acc[2][3];
  const uintptr_t wb = (uintptr_t)((wr ? w3 : w1) + ct * 96), xb = (uintptr_t)ws + WS_XN + (w * S_ + rt * 128) * 16;
  if (GATE) { while (__builtin_amdgcn_s_memrealtime() - t0 < GATE) __builtin_amdgcn_s_sleep(2); }
  else { while (__builtin_amdgcn_s_memrealtime() - t0 < TGF) __builtin_amdgcn_s_sleep(2); await(ws, gate, ~0u, tag, w); }
  __builtin_amdgcn_s_setprio(PRIO_GU);
  STAMP(ws, 0, 2);
#define GDBG_OP
  asm volatile(
      "s_lshl_b32 s40, %[k0], 18\n"
      "s_mul_i32 s42, %[k0], 196608\n"
      "s_add_u32 m0, %[ldx], 0\n"
      "s_add_u32 s44, %[xb0], s40\n"
      "s_addc_u32 s45, %[xb1], 0\n"
      "global_load_lds_dwordx4 %[xv], s[44:45] sc1\n"
      "global_load_lds_dwordx4 %[xv], s[44:45] offset:1024 sc1\n"
      "s_add_u32 s40, s40, 131072\n"
      "s_add_u32 m0, %[ldw], 0\n"
      "s_add_u32 s44, %[wb0], s42\n"
      "s_addc_u32 s45, %[wb1], 0\n"
      "global_load_lds_dwordx4 %[wv0], s[44:45]\n"
      "s_add_u32 m0, m0, 0x400\n"
      "s_add_u32 s42, s42, 98304\n"
      "global_load_lds_dwordx4 %[wv1], s[44:45]\n"
      "s_add_u32 m0, m0, 0x400\n"
      "s_cmp_eq_u32 s42, 1966080\n"
      "global_load_lds_dwordx4 %[wv2], s[44:45]\n"
      "s_cselect_b32 s40, 0, s40\n"
      "s_cselect_b32 s42, 0, s42\n"
      "s_add_u32 m0, %[ldx], 8192\n"
      "s_add_u32 s44, %[xb0], s40\n"
      "s_addc_u32 s45, %[xb1], 0\n"
      "global_load_lds_dwordx4 %[xv], s[44:45] sc1\n"
      "global_load_lds_dwordx4 %[xv], s[44:45] offset:1024 sc1\n"
      "s_add_u32 s40, s40, 131072\n"
      "s_add_u32 m0, %[ldw], 6144\n"
      "s_add_u32 s44, %[wb0], s42\n"
      "s_addc_u32 s45, %[wb1], 0\n"
      "global_load_lds_dwordx4 %[wv0], s[44:45]\n"
      "s_add_u32 m0, m0, 0x400\n"
      "s_add_u32 s42, s42, 98304\n"
      "global_load_lds_dwordx4 %[wv1], s[44:45]\n"
      "s_add_u32 m0, m0, 0x400\n"
      "s_cmp_eq_u32 s42, 1966080\n"
      "global_load_lds_dwordx4 %[wv2], s[44:45]\n"
      "s_cselect_b32 s40, 0, s40\n"
      "s_cselect_b32 s42, 0, s42\n"
      "s_add_u32 m0, %[ldx], 16384\n"
      "s_add_u32 s44, %[xb0], s40\n"
      "s_addc_u32 s45, %[xb1], 0\n"
      "global_load_lds_dwordx4 %[xv], s[44:45] sc1\n"
      "global_load_lds_dwordx4 %[xv], s[44:45] offset:1024 sc1\n"
      "s_add_u32 s40, s40, 131072\n"
      "s_add_u32 m0, %[ldw], 12288\n"
      "s_add_u32 s44, %[wb0], s42\n"
      "s_addc_u32 s45, %[wb1], 0\n"
      "global_load_lds_dwordx4 %[wv0], s[44:45]\n"
      "s_add_u32 m0, m0, 0x400\n"
      "s_add_u32 s42, s42, 98304\n"
      "global_load_lds_dwordx4 %[wv1], s[44:45]\n"
      "s_add_u32 m0, m0, 0x400\n"
      "s_cmp_eq_u32 s42, 1966080\n"
      "global_load_lds_dwordx4 %[wv2], s[44:45]\n"
      "s_cselect_b32 s40, 0, s40\n"
      "s_cselect_b32 s42, 0, s42\n"
      "s_mov_b32 s36, 3\n"
      "s_waitcnt vmcnt(5)\n"
      "s_barrier\n"
      "ds_read_b128 v[16:19], %[xa] offset:0\n"
      "ds_read_b64_tr_b16 v[24:25], %[wa0] offset:0\n"
      "ds_read_b64_tr_b16 v[26:27], %[wa0] offset:768\n"
      "ds_read_b64_tr_b16 v[28:29], %[wa1] offset:0\n"
      "ds_read_b64_tr_b16 v[30:31], %[wa1] offset:768\n"
      "ds_read_b64_tr_b16 v[32:33], %[wa2] offset:0\n"
      "ds_read_b64_tr_b16 v[34:35], %[wa2] offset:768\n"
      "ds_read_b128 v[20:23], %[xa] offset:512\n"
      "s_waitcnt lgkmcnt(5)\n"
      "v_mfma_f32_32x32x16_bf16 v[176:191], v[24:27], v[16:19], 0\n"
      "ds_read_b128 v[36:39], %[xa] offset:4096\n"
      "ds_read_b64_tr_b16 v[44:45], %[wa0] offset:3072\n"
      "ds_read_b64_tr_b16 v[46:47], %[wa0] offset:3840\n"
      "ds_read_b64_tr_b16 v[48:49], %[wa1] offset:3072\n"
      "s_waitcnt lgkmcnt(7)\n"
      "v_mfma_f32_32x32x16_bf16 v[192:207], v[28:31], v[16:19], 0\n"
      "ds_read_b64_tr_b16 v[50:51], %[wa1] offset:3840\n"
      "ds_read_b64_tr_b16 v[52:53], %[wa2] offset:3072\n"
      "ds_read_b64_tr_b16 v[54:55], %[wa2] offset:3840\n"
      "ds_read_b128 v[40:43], %[xa] offset:4608\n"
      "s_add_u32 m0, %[ldx], 24576\n"
      "s_add_u32 s44, %[xb0], s40\n"
      "s_addc_u32 s45, %[xb1], 0\n"
      "global_load_lds_dwordx4 %[xv], s[44:45] sc1\n"
      "s_waitcnt lgkmcnt(9)\n"
      "v_mfma_f32_32x32x16_bf16 v[208:223], v[32:35], v[16:19], 0\n"
      "global_load_lds_dwordx4 %[xv], s[44:45] offset:1024 sc1\n"
      "s_add_u32 s40, s40, 131072\n"
      "s_waitcnt lgkmcnt(8)\n"
      "v_mfma_f32_32x32x16_bf16 v[60:75], v[32:35], v[20:23], 0\n"
      "s_add_u32 m0, %[ldw], 18432\n"
      "s_add_u32 s44, %[wb0], s42\n"
      "s_addc_u32 s45, %[wb1], 0\n"
      "global_load_lds_dwordx4 %[wv0], s[44:45]\n"
      "v_mfma_f32_32x32x16_bf16 v[240:255], v[28:31], v[20:23], 0\n"
      "s_add_u32 m0, m0, 0x400\n"
      "s_add_u32 s42, s42, 98304\n"
      "global_load_lds_dwordx4 %[wv1], s[44:45]\n"
      "v_mfma_f32_32x32x16_bf16 v[224:239], v[24:27], v[20:23], 0\n"
      "s_add_u32 m0, m0, 0x400\n"
      "s_cmp_eq_u32 s42, 1966080\n"
      "global_load_lds_dwordx4 %[wv2], s[44:45]\n"
      "s_cselect_b32 s40, 0, s40\n"
      "s_cselect_b32 s42, 0, s42\n"
      "s_waitcnt lgkmcnt(5)\n"
      "v_mfma_f32_32x32x16_bf16 v[176:191], v[44:47], v[36:39], v[176:191]\n"
      "ds_read_b128 v[16:19], %[xa] offset:8192\n"
      "ds_read_b64_tr_b16 v[24:25], %[wa0] offset:6144\n"
      "ds_read_b64_tr_b16 v[26:27], %[wa0] offset:6912\n"
      "ds_read_b64_tr_b16 v[28:29], %[wa1] offset:6144\n"
      "s_waitcnt lgkmcnt(7)\n"
      "v_mfma_f32_32x32x16_bf16 v[192:207], v[48:51], v[36:39], v[192:207]\n"
      "ds_read_b64_tr_b16 v[30:31], %[wa1] offset:6912\n"
      "ds_read_b64_tr_b16 v[32:33], %[wa2] offset:6144\n"
      "ds_read_b64_tr_b16 v[34:35], %[wa2] offset:6912\n"
      "ds_read_b128 v[20:23], %[xa] offset:8704\n"
      "s_waitcnt lgkmcnt(9)\n"
      "v_mfma_f32_32x32x16_bf16 v[208:223], v[52:55], v[36:39], v[208:223]\n"
      "s_waitcnt lgkmcnt(8)\n"
      "v_mfma_f32_32x32x16_bf16 v[60:75], v[52:55], v[40:43], v[60:75]\n"
      "v_mfma_f32_32x32x16_bf16 v[240:255], v[48:51], v[40:43], v[240:255]\n"
      "v_mfma_f32_32x32x16_bf16 v[224:239], v[44:47], v[40:43], v[224:239]\n"
      "s_waitcnt lgkmcnt(5)\n"
      "v_mfma_f32_32x32x16_bf16 v[176:191], v[24:27], v[16:19], v[176:191]\n"
      "ds_read_b128 v[36:39], %[xa] offset:12288\n"
      "ds_read_b64_tr_b16 v[44:45], %[wa0] offset:9216\n"
      "ds_read_b64_tr_b16 v[46:47], %[wa0] offset:9984\n"
      "ds_read_b64_tr_b16 v[48:49], %[wa1] offset:9216\n"
      "s_waitcnt lgkmcnt(7)\n"
      "v_mfma_f32_32x32x16_bf16 v[192:207], v[28:31], v[16:19], v[192:207]\n"
      "ds_read_b64_tr_b16 v[50:51], %[wa1] offset:9984\n"
      "ds_read_b64_tr_b16 v[52:53], %[wa2] offset:9216\n"
      "ds_read_b64_tr_b16 v[54:55], %[wa2] offset:9984\n"
      "ds_read_b128 v[40:43], %[xa] offset:12800\n"
      "s_waitcnt lgkmcnt(9)\n"
      "v_mfma_f32_32x32x16_bf16 v[208:223], v[32:35], v[16:19], v[208:223]\n"
      "s_waitcnt lgkmcnt(8)\n"
      "v_mfma_f32_32x32x16_bf16 v[60:75], v[32:35], v[20:23], v[60:75]\n"
      "v_mfma_f32_32x32x16_bf16 v[240:255], v[28:31], v[20:23], v[240:255]\n"
      "v_mfma_f32_32x32x16_bf16 v[224:239], v[24:27], v[20:23], v[224:239]\n"
      "s_waitcnt vmcnt(0) lgkmcnt(0)\n"
      "s_barrier\n"
      "v_mfma_f32_32x32x16_bf16 v[176:191], v[44:47], v[36:39], v[176:191]\n"
      "ds_read_b128 v[16:19], %[xa] offset:16384\n"
      "ds_read_b64_tr_b16 v[24:25], %[wa0] offset:12288\n"
      "ds_read_b64_tr_b16 v[26:27], %[wa0] offset:13056\n"
      "ds_read_b64_tr_b16 v[28:29], %[wa1] offset:12288\n"
      "v_mfma_f32_32x32x16_bf16 v[192:207], v[48:51], v[36:39], v[192:207]\n"
      "ds_read_b64_tr_b16 v[30:31], %[wa1] offset:13056\n"
      "ds_read_b64_tr_b16 v[32:33], %[wa2] offset:12288\n"
      "ds_read_b64_tr_b16 v[34:35], %[wa2] offset:13056\n"
      "ds_read_b128 v[20:23], %[xa] offset:16896\n"
      "s_add_u32 m0, %[ldx], 0\n"
      "s_add_u32 s44, %[xb0], s40\n"
      "s_addc_u32 s45, %[xb1], 0\n"
      "global_load_lds_dwordx4 %[xv], s[44:45] sc1\n"
      "v_mfma_f32_32x32x16_bf16 v[208:223], v[52:55], v[36:39], v[208:223]\n"
      "global_load_lds_dwordx4 %[xv], s[44:45] offset:1024 sc1\n"
      "s_add_u32 s40, s40, 131072\n"
      "v_mfma_f32_32x32x16_bf16 v[60:75], v[52:55], v[40:43], v[60:75]\n"
      "s_add_u32 m0, %[ldw], 0\n"
      "s_add_u32 s44, %[wb0], s42\n"
      "s_addc_u32 s45, %[wb1], 0\n"
      "global_load_lds_dwordx4 %[wv0], s[44:45]\n"
      "v_mfma_f32_32x32x16_bf16 v[240:255], v[48:51], v[40:43], v[240:255]\n"
      "s_add_u32 m0, m0, 0x400\n"
      "s_add_u32 s42, s42, 98304\n"
      "global_load_lds_dwordx4 %[wv1], s[44:45]\n"
      "v_mfma_f32_32x32x16_bf16 v[224:239], v[44:47], v[40:43], v[224:239]\n"
      "s_add_u32 m0, m0, 0x400\n"
      "s_cmp_eq_u32 s42, 1966080\n"
      "global_load_lds_dwordx4 %[wv2], s[44:45]\n"
      "s_cselect_b32 s40, 0, s40\n"
      "s_cselect_b32 s42, 0, s42\n"
      "s_waitcnt lgkmcnt(5)\n"
      "v_mfma_f32_32x32x16_bf16 v[176:191], v[24:27], v[16:19], v[176:191]\n"
      "ds_read_b128 v[36:39], %[xa] offset:20480\n"
      "ds_read_b64_tr_b16 v[44:45], %[wa0] offset:15360\n"
      "ds_read_b64_tr_b16 v[46:47], %[wa0] offset:16128\n"
      "ds_read_b64_tr_b16 v[48:49], %[wa1] offset:15360\n"
      "s_waitcnt lgkmcnt(7)\n"
      "v_mfma_f32_32x32x16_bf16 v[192:207], v[28:31], v[16:19], v[192:207]\n"
      "ds_read_b64_tr_b16 v[50:51], %[wa1] offset:16128\n"
      "ds_read_b64_tr_b16 v[52:53], %[wa2] offset:15360\n"
      "ds_read_b64_tr_b16 v[54:55], %[wa2] offset:16128\n"
      "ds_read_b128 v[40:43], %[xa] offset:20992\n"
      "s_add_u32 m0, %[ldx], 8192\n"
      "s_add_u32 s44, %[xb0], s40\n"
      "s_addc_u32 s45, %[xb1], 0\n"
      "global_load_lds_dwordx4 %[xv], s[44:45] sc1\n"
      "s_waitcnt lgkmcnt(9)\n"
      "v_mfma_f32_32x32x16_bf16 v[208:223], v[32:35], v[16:19], v[208:223]\n"
      "global_load_lds_dwordx4 %[xv], s[44:45] offset:1024 sc1\n"
      "s_add_u32 s40, s40, 131072\n"
      "s_waitcnt lgkmcnt(8)\n"
      "v_mfma_f32_32x32x16_bf16 v[60:75], v[32:35], v[20:23], v[60:75]\n"
      "s_add_u32 m0, %[ldw], 6144\n"
      "s_add_u32 s44, %[wb0], s42\n"
      "s_addc_u32 s45, %[wb1], 0\n"
      "global_load_lds_dwordx4 %[wv0], s[44:45]\n"
      "v_mfma_f32_32x32x16_bf16 v[240:255], v[28:31], v[20:23], v[240:255]\n"
      "s_add_u32 m0, m0, 0x400\n"
      "s_add_u32 s42, s42, 98304\n"
      "global_load_lds_dwordx4 %[wv1], s[44:45]\n"
      "v_mfma_f32_32x32x16_bf16 v[224:239], v[24:27], v[20:23], v[224:239]\n"
      "s_add_u32 m0, m0, 0x400\n"
      "s_cmp_eq_u32 s42, 1966080\n"
      "global_load_lds_dwordx4 %[wv2], s[44:45]\n"
      "s_cselect_b32 s40, 0, s40\n"
      "s_cselect_b32 s42, 0, s42\n"
      "s_waitcnt lgkmcnt(5)\n"
      "v_mfma_f32_32x32x16_bf16 v[176:191], v[44:47], v[36:39], v[176:191]\n"
      "ds_read_b128 v[16:19], %[xa] offset:24576\n"
      "ds_read_b64_tr_b16 v[24:25], %[wa0] offset:18432\n"
      "ds_read_b64_tr_b16 v[26:27], %[wa0] offset:19200\n"
      "ds_read_b64_tr_b16 v[28:29], %[wa1] offset:18432\n"
      "s_waitcnt lgkmcnt(7)\n"
      "v_mfma_f32_32x32x16_bf16 v[192:207], v[48:51], v[36:39], v[192:207]\n"
      "ds_read_b64_tr_b16 v[30:31], %[wa1] offset:19200\n"
      "ds_read_b64_tr_b16 v[32:33], %[wa2] offset:18432\n"
      "ds_read_b64_tr_b16 v[34:35], %[wa2] offset:19200\n"
      "ds_read_b128 v[20:23], %[xa] offset:25088\n"
      "s_waitcnt lgkmcnt(9)\n"
      "v_mfma_f32_32x32x16_bf16 v[208:223], v[52:55], v[36:39], v[208:223]\n"
      "s_waitcnt lgkmcnt(8)\n"
      "v_mfma_f32_32x32x16_bf16 v[60:75], v[52:55], v[40:43], v[60:75]\n"
      "v_mfma_f32_32x32x16_bf16 v[240:255], v[48:51], v[40:43], v[240:255]\n"
      "v_mfma_f32_32x32x16_bf16 v[224:239], v[44:47], v[40:43], v[224:239]\n"
      "s_waitcnt lgkmcnt(5)\n"
      "v_mfma_f32_32x32x16_bf16 v[176:191], v[24:27], v[16:19], v[176:191]\n"
      "ds_read_b128 v[36:39], %[xa] offset:28672\n"
      "ds_read_b64_tr_b16 v[44:45], %[wa0] offset:21504\n"
      "ds_read_b64_tr_b16 v[46:47], %[wa0] offset:22272\n"
      "ds_read_b64_tr_b16 v[48:49], %[wa1] offset:21504\n"
      "s_waitcnt lgkmcnt(7)\n"
      "v_mfma_f32_32x32x16_bf16 v[192:207], v[28:31], v[16:19], v[192:207]\n"
      "ds_read_b64_tr_b16 v[50:51], %[wa1] offset:22272\n"
      "ds_read_b64_tr_b16 v[52:53], %[wa2] offset:21504\n"
      "ds_read_b64_tr_b16 v[54:55], %[wa2] offset:22272\n"
      "ds_read_b128 v[40:43], %[xa] offset:29184\n"
      "s_waitcnt lgkmcnt(9)\n"
      "v_mfma_f32_32x32x16_bf16 v[208:223], v[32:35], v[16:19], v[208:223]\n"
      "s_waitcnt lgkmcnt(8)\n"
      "v_mfma_f32_32x32x16_bf16 v[60:75], v[32:35], v[20:23], v[60:75]\n"
      "v_mfma_f32_32x32x16_bf16 v[240:255], v[28:31], v[20:23], v[240:255]\n"
      "v_mfma_f32_32x32x16_bf16 v[224:239], v[24:27], v[20:23], v[224:239]\n"
      "s_waitcnt vmcnt(0) lgkmcnt(0)\n"
      "s_barrier\n"
      "v_mfma_f32_32x32x16_bf16 v[176:191], v[44:47], v[36:39], v[176:191]\n"
      "ds_read_b128 v[16:19], %[xa] offset:0\n"
      "ds_read_b64_tr_b16 v[24:25], %[wa0] offset:0\n"
      "ds_read_b64_tr_b16 v[26:27], %[wa0] offset:768\n"
      "ds_read_b64_tr_b16 v[28:29], %[wa1] offset:0\n"
      "v_mfma_f32_32x32x16_bf16 v[192:207], v[48:51], v[36:39], v[192:207]\n"
      "ds_read_b64_tr_b16 v[30:31], %[wa1] offset:768\n"
      "ds_read_b64_tr_b16 v[32:33], %[wa2] offset:0\n"
      "ds_read_b64_tr_b16 v[34:35], %[wa2] offset:768\n"
      "ds_read_b128 v[20:23], %[xa] offset:512\n"
      "s_add_u32 m0, %[ldx], 16384\n"
      "s_add_u32 s44, %[xb0], s40\n"
      "s_addc_u32 s45, %[xb1], 0\n"
      "global_load_lds_dwordx4 %[xv], s[44:45] sc1\n"
      "v_mfma_f32_32x32x16_bf16 v[208:223], v[52:55], v[36:39], v[208:223]\n"
      "global_load_lds_dwordx4 %[xv], s[44:45] offset:1024 sc1\n"
      "s_add_u32 s40, s40, 131072\n"
      "v_mfma_f32_32x32x16_bf16 v[60:75], v[52:55], v[40:43], v[60:75]\n"
      "s_add_u32 m0, %[ldw], 12288\n"
      "s_add_u32 s44, %[wb0], s42\n"
      "s_addc_u32 s45, %[wb1], 0\n"
      "global_load_lds_dwordx4 %[wv0], s[44:45]\n"
      "v_mfma_f32_32x32x16_bf16 v[240:255], v[48:51], v[40:43], v[240:255]\n"
      "s_add_u32 m0, m0, 0x400\n"
      "s_add_u32 s42, s42, 98304\n"
      "global_load_lds_dwordx4 %[wv1], s[44:45]\n"
      "v_mfma_f32_32x32x16_bf16 v[224:239], v[44:47], v[40:43], v[224:239]\n"
      "s_add_u32 m0, m0, 0x400\n"
      "s_cmp_eq_u32 s42, 1966080\n"
      "global_load_lds_dwordx4 %[wv2], s[44:45]\n"
      "s_cselect_b32 s40, 0, s40\n"
      "s_cselect_b32 s42, 0, s42\n"
      ".p2align 3\n"
      "LOOP_%=:\n"
      "s_waitcnt lgkmcnt(5)\n"
      "v_mfma_f32_32x32x16_bf16 v[176:191], v[24:27], v[16:19], v[176:191]\n"
      "ds_read_b128 v[36:39], %[xa] offset:4096\n"
      "ds_read_b64_tr_b16 v[44:45], %[wa0] offset:3072\n"
      "ds_read_b64_tr_b16 v[46:47], %[wa0] offset:3840\n"
      "ds_read_b64_tr_b16 v[48:49], %[wa1] offset:3072\n"
      "s_waitcnt lgkmcnt(7)\n"
      "v_mfma_f32_32x32x16_bf16 v[192:207], v[28:31], v[16:19], v[192:207]\n"
      "ds_read_b64_tr_b16 v[50:51], %[wa1] offset:3840\n"
      "ds_read_b64_tr_b16 v[52:53], %[wa2] offset:3072\n"
      "ds_read_b64_tr_b16 v[54:55], %[wa2] offset:3840\n"
      "ds_read_b128 v[40:43], %[xa] offset:4608\n"
      "s_add_u32 m0, %[ldx], 24576\n"
      "s_add_u32 s44, %[xb0], s40\n"
      "s_addc_u32 s45, %[xb1], 0\n"
      "global_load_lds_dwordx4 %[xv], s[44:45] sc1\n"
      "s_waitcnt lgkmcnt(9)\n"
      "v_mfma_f32_32x32x16_bf16 v[208:223], v[32:35], v[16:19], v[208:223]\n"
      "global_load_lds_dwordx4 %[xv], s[44:45] offset:1024 sc1\n"
      "s_add_u32 s40, s40, 131072\n"
      "s_waitcnt lgkmcnt(8)\n"
      "v_mfma_f32_32x32x16_bf16 v[60:75], v[32:35], v[20:23], v[60:75]\n"
      "s_add_u32 m0, %[ldw], 18432\n"
      "s_add_u32 s44, %[wb0], s42\n"
      "s_addc_u32 s45, %[wb1], 0\n"
      "global_load_lds_dwordx4 %[wv0], s[44:45]\n"
      "v_mfma_f32_32x32x16_bf16 v[240:255], v[28:31], v[20:23], v[240:255]\n"
      "s_add_u32 m0, m0, 0x400\n"
      "s_add_u32 s42, s42, 98304\n"
      "global_load_lds_dwordx4 %[wv1], s[44:45]\n"
      "v_mfma_f32_32x32x16_bf16 v[224:239], v[24:27], v[20:23], v[224:239]\n"
      "s_add_u32 m0, m0, 0x400\n"
      "s_cmp_eq_u32 s42, 1966080\n"
      "global_load_lds_dwordx4 %[wv2], s[44:45]\n"
      "s_cselect_b32 s40, 0, s40\n"
      "s_cselect_b32 s42, 0, s42\n"
      "s_waitcnt lgkmcnt(5)\n"
      "v_mfma_f32_32x32x16_bf16 v[176:191], v[44:47], v[36:39], v[176:191]\n"
      "ds_read_b128 v[16:19], %[xa] offset:8192\n"
      "ds_read_b64_tr_b16 v[24:25], %[wa0] offset:6144\n"
      "ds_read_b64_tr_b16 v[26:27], %[wa0] offset:6912\n"
      "ds_read_b64_tr_b16 v[28:29], %[wa1] offset:6144\n"
      "s_waitcnt lgkmcnt(7)\n"
      "v_mfma_f32_32x32x16_bf16 v[192:207], v[48:51], v[36:39], v[192:207]\n"
      "ds_read_b64_tr_b16 v[30:31], %[wa1] offset:6912\n"
      "ds_read_b64_tr_b16 v[32:33], %[wa2] offset:6144\n"
      "ds_read_b64_tr_b16 v[34:35], %[wa2] offset:6912\n"
      "ds_read_b128 v[20:23], %[xa] offset:8704\n"
      "s_waitcnt lgkmcnt(9)\n"
      "v_mfma_f32_32x32x16_bf16 v[208:223], v[52:55], v[36:39], v[208:223]\n"
      "s_waitcnt lgkmcnt(8)\n"
      "v_mfma_f32_32x32x16_bf16 v[60:75], v[52:55], v[40:43], v[60:75]\n"
      "v_mfma_f32_32x32x16_bf16 v[240:255], v[48:51], v[40:43], v[240:255]\n"
      "v_mfma_f32_32x32x16_bf16 v[224:239], v[44:47], v[40:43], v[224:239]\n"
      "s_waitcnt lgkmcnt(5)\n"
      "v_mfma_f32_32x32x16_bf16 v[176:191], v[24:27], v[16:19], v[176:191]\n"
      "ds_read_b128 v[36:39], %[xa] offset:12288\n"
      "ds_read_b64_tr_b16 v[44:45], %[wa0] offset:9216\n"
      "ds_read_b64_tr_b16 v[46:47], %[wa0] offset:9984\n"
      "ds_read_b64_tr_b16 v[48:49], %[wa1] offset:9216\n"
      "s_waitcnt lgkmcnt(7)\n"
      "v_mfma_f32_32x32x16_bf16 v[192:207], v[28:31], v[16:19], v[192:207]\n"
      "ds_read_b64_tr_b16 v[50:51], %[wa1] offset:9984\n"
      "ds_read_b64_tr_b16 v[52:53], %[wa2] offset:9216\n"
      "ds_read_b64_tr_b16 v[54:55], %[wa2] offset:9984\n"
      "ds_read_b128 v[40:43], %[xa] offset:12800\n"
      "s_waitcnt lgkmcnt(9)\n"
      "v_mfma_f32_32x32x16_bf16 v[208:223], v[32:35], v[16:19], v[208:223]\n"
      "s_waitcnt lgkmcnt(8)\n"
      "v_mfma_f32_32x32x16_bf16 v[60:75], v[32:35], v[20:23], v[60:75]\n"
      "v_mfma_f32_32x32x16_bf16 v[240:255], v[28:31], v[20:23], v[240:255]\n"
      "v_mfma_f32_32x32x16_bf16 v[224:239], v[24:27], v[20:23], v[224:239]\n"
      "s_waitcnt vmcnt(0) lgkmcnt(0)\n"
      "s_barrier\n"
      "v_mfma_f32_32x32x16_bf16 v[176:191], v[44:47], v[36:39], v[176:191]\n"
      "ds_read_b128 v[16:19], %[xa] offset:16384\n"
      "ds_read_b64_tr_b16 v[24:25], %[wa0] offset:12288\n"
      "ds_read_b64_tr_b16 v[26:27], %[wa0] offset:13056\n"
      "ds_read_b64_tr_b16 v[28:29], %[wa1] offset:12288\n"
      "v_mfma_f32_32x32x16_bf16 v[192:207], v[48:51], v[36:39], v[192:207]\n"
      "ds_read_b64_tr_b16 v[30:31], %[wa1] offset:13056\n"
      "ds_read_b64_tr_b16 v[32:33], %[wa2] offset:12288\n"
      "ds_read_b64_tr_b16 v[34:35], %[wa2] offset:13056\n"
      "ds_read_b128 v[20:23], %[xa] offset:16896\n"
      "s_add_u32 m0, %[ldx], 0\n"
      "s_add_u32 s44, %[xb0], s40\n"
      "s_addc_u32 s45, %[xb1], 0\n"
      "global_load_lds_dwordx4 %[xv], s[44:45] sc1\n"
      "v_mfma_f32_32x32x16_bf16 v[208:223], v[52:55], v[36:39], v[208:223]\n"
      "global_load_lds_dwordx4 %[xv], s[44:45] offset:1024 sc1\n"
      "s_add_u32 s40, s40, 131072\n"
      "v_mfma_f32_32x32x16_bf16 v[60:75], v[52:55], v[40:43], v[60:75]\n"
      "s_add_u32 m0, %[ldw], 0\n"
      "s_add_u32 s44, %[wb0], s42\n"
      "s_addc_u32 s45, %[wb1], 0\n"
      "global_load_lds_dwordx4 %[wv0], s[44:45]\n"
      "v_mfma_f32_32x32x16_bf16 v[240:255], v[48:51], v[40:43], v[240:255]\n"
      "s_add_u32 m0, m0, 0x400\n"
      "s_add_u32 s42, s42, 98304\n"
      "global_load_lds_dwordx4 %[wv1], s[44:45]\n"
      "v_mfma_f32_32x32x16_bf16 v[224:239], v[44:47], v[40:43], v[224:239]\n"
      "s_add_u32 m0, m0, 0x400\n"
      "s_cmp_eq_u32 s42, 1966080\n"
      "global_load_lds_dwordx4 %[wv2], s[44:45]\n"
      "s_cselect_b32 s40, 0, s40\n"
      "s_cselect_b32 s42, 0, s42\n"
      "s_waitcnt lgkmcnt(5)\n"
      "v_mfma_f32_32x32x16_bf16 v[176:191], v[24:27], v[16:19], v[176:191]\n"
      "ds_read_b128 v[36:39], %[xa] offset:20480\n"
      "ds_read_b64_tr_b16 v[44:45], %[wa0] offset:15360\n"
      "ds_read_b64_tr_b16 v[46:47], %[wa0] offset:16128\n"
      "ds_read_b64_tr_b16 v[48:49], %[wa1] offset:15360\n"
      "s_waitcnt lgkmcnt(7)\n"
      "v_mfma_f32_32x32x16_bf16 v[192:207], v[28:31], v[16:19], v[192:207]\n"
      "ds_read_b64_tr_b16 v[50:51], %[wa1] offset:16128\n"
      "ds_read_b64_tr_b16 v[52:53], %[wa2] offset:15360\n"
      "ds_read_b64_tr_b16 v[54:55], %[wa2] offset:16128\n"
      "ds_read_b128 v[40:43], %[xa] offset:20992\n"
      "s_add_u32 m0, %[ldx], 8192\n"
      "s_add_u32 s44, %[xb0], s40\n"
      "s_addc_u32 s45, %[xb1], 0\n"
      "global_load_lds_dwordx4 %[xv], s[44:45] sc1\n"
      "s_waitcnt lgkmcnt(9)\n"
      "v_mfma_f32_32x32x16_bf16 v[208:223], v[32:35], v[16:19], v[208:223]\n"
      "global_load_lds_dwordx4 %[xv], s[44:45] offset:1024 sc1\n"
      "s_add_u32 s40, s40, 131072\n"
      "s_waitcnt lgkmcnt(8)\n"
      "v_mfma_f32_32x32x16_bf16 v[60:75], v[32:35], v[20:23], v[60:75]\n"
      "s_add_u32 m0, %[ldw], 6144\n"
      "s_add_u32 s44, %[wb0], s42\n"
      "s_addc_u32 s45, %[wb1], 0\n"
      "global_load_lds_dwordx4 %[wv0], s[44:45]\n"
      "v_mfma_f32_32x32x16_bf16 v[240:255], v[28:31], v[20:23], v[240:255]\n"
      "s_add_u32 m0, m0, 0x400\n"
      "s_add_u32 s42, s42, 98304\n"
      "global_load_lds_dwordx4 %[wv1], s[44:45]\n"
      "v_mfma_f32_32x32x16_bf16 v[224:239], v[24:27], v[20:23], v[224:239]\n"
      "s_add_u32 m0, m0, 0x400\n"
      "s_cmp_eq_u32 s42, 1966080\n"
      "global_load_lds_dwordx4 %[wv2], s[44:45]\n"
      "s_cselect_b32 s40, 0, s40\n"
      "s_cselect_b32 s42, 0, s42\n"
      "s_waitcnt lgkmcnt(5)\n"
      "v_mfma_f32_32x32x16_bf16 v[176:191], v[44:47], v[36:39], v[176:191]\n"
      "ds_read_b128 v[16:19], %[xa] offset:24576\n"
      "ds_read_b64_tr_b16 v[24:25], %[wa0] offset:18432\n"
      "ds_read_b64_tr_b16 v[26:27], %[wa0] offset:19200\n"
      "ds_read_b64_tr_b16 v[28:29], %[wa1] offset:18432\n"
      "s_waitcnt lgkmcnt(7)\n"
      "v_mfma_f32_32x32x16_bf16 v[192:207], v[48:51], v[36:39], v[192:207]\n"
      "ds_read_b64_tr_b16 v[30:31], %[wa1] offset:19200\n"
      "ds_read_b64_tr_b16 v[32:33], %[wa2] offset:18432\n"
      "ds_read_b64_tr_b16 v[34:35], %[wa2] offset:19200\n"
      "ds_read_b128 v[20:23], %[xa] offset:25088\n"
      "global_load_dwordx2 v[56:57], %[fz], %[ws] sc1\n"
      "s_waitcnt lgkmcnt(9)\n"
      "v_mfma_f32_32x32x16_bf16 v[208:223], v[52:55], v[36:39], v[208:223]\n"
      "s_waitcnt lgkmcnt(8)\n"
      "v_mfma_f32_32x32x16_bf16 v[60:75], v[52:55], v[40:43], v[60:75]\n"
      "v_mfma_f32_32x32x16_bf16 v[240:255], v[48:51], v[40:43], v[240:255]\n"
      "v_mfma_f32_32x32x16_bf16 v[224:239], v[44:47], v[40:43], v[224:239]\n"
      "s_waitcnt lgkmcnt(5)\n"
      "v_mfma_f32_32x32x16_bf16 v[176:191], v[24:27], v[16:19], v[176:191]\n"
      "ds_read_b128 v[36:39], %[xa] offset:28672\n"
      "ds_read_b64_tr_b16 v[44:45], %[wa0] offset:21504\n"
      "ds_read_b64_tr_b16 v[46:47], %[wa0] offset:22272\n"
      "ds_read_b64_tr_b16 v[48:49], %[wa1] offset:21504\n"
      "s_waitcnt lgkmcnt(7)\n"
      "v_mfma_f32_32x32x16_bf16 v[192:207], v[28:31], v[16:19], v[192:207]\n"
      "ds_read_b64_tr_b16 v[50:51], %[wa1] offset:22272\n"
      "ds_read_b64_tr_b16 v[52:53], %[wa2] offset:21504\n"
      "ds_read_b64_tr_b16 v[54:55], %[wa2] offset:22272\n"
      "ds_read_b128 v[40:43], %[xa] offset:29184\n"
      "s_waitcnt lgkmcnt(9)\n"
      "v_mfma_f32_32x32x16_bf16 v[208:223], v[32:35], v[16:19], v[208:223]\n"
      "s_waitcnt lgkmcnt(8)\n"
      "v_mfma_f32_32x32x16_bf16 v[60:75], v[32:35], v[20:23], v[60:75]\n"
      "v_mfma_f32_32x32x16_bf16 v[240:255], v[28:31], v[20:23], v[240:255]\n"
      "v_mfma_f32_32x32x16_bf16 v[224:239], v[24:27], v[20:23], v[224:239]\n"
      "s_waitcnt vmcnt(0) lgkmcnt(0)\n"
      "s_barrier\n"
      "v_mfma_f32_32x32x16_bf16 v[176:191], v[44:47], v[36:39], v[176:191]\n"
      "ds_read_b128 v[16:19], %[xa] offset:0\n"
      "ds_read_b64_tr_b16 v[24:25], %[wa0] offset:0\n"
      "ds_read_b64_tr_b16 v[26:27], %[wa0] offset:768\n"
      "ds_read_b64_tr_b16 v[28:29], %[wa1] offset:0\n"
      "v_mfma_f32_32x32x16_bf16 v[192:207], v[48:51], v[36:39], v[192:207]\n"
      "ds_read_b64_tr_b16 v[30:31], %[wa1] offset:768\n"
      "ds_read_b64_tr_b16 v[32:33], %[wa2] offset:0\n"
      "ds_read_b64_tr_b16 v[34:35], %[wa2] offset:768\n"
      "ds_read_b128 v[20:23], %[xa] offset:512\n"
      "s_add_u32 m0, %[ldx], 16384\n"
      "s_add_u32 s44, %[xb0], s40\n"
      "s_addc_u32 s45, %[xb1], 0\n"
      "global_load_lds_dwordx4 %[xv], s[44:45] sc1\n"
      "v_mfma_f32_32x32x16_bf16 v[208:223], v[52:55], v[36:39], v[208:223]\n"
      "global_load_lds_dwordx4 %[xv], s[44:45] offset:1024 sc1\n"
      "s_add_u32 s40, s40, 131072\n"
      "v_mfma_f32_32x32x16_bf16 v[60:75], v[52:55], v[40:43], v[60:75]\n"
      "s_add_u32 m0, %[ldw], 12288\n"
      "s_add_u32 s44, %[wb0], s42\n"
      "s_addc_u32 s45, %[wb1], 0\n"
      "global_load_lds_dwordx4 %[wv0], s[44:45]\n"
      "v_mfma_f32_32x32x16_bf16 v[240:255], v[48:51], v[40:43], v[240:255]\n"
      "s_add_u32 m0, m0, 0x400\n"
      "s_add_u32 s42, s42, 98304\n"
      "global_load_lds_dwordx4 %[wv1], s[44:45]\n"
      "v_mfma_f32_32x32x16_bf16 v[224:239], v[44:47], v[40:43], v[224:239]\n"
      "s_add_u32 m0, m0, 0x400\n"
      "s_cmp_eq_u32 s42, 1966080\n"
      "global_load_lds_dwordx4 %[wv2], s[44:45]\n"
      "s_cselect_b32 s40, 0, s40\n"
      "s_cselect_b32 s42, 0, s42\n"
      "s_sub_u32 s36, s36, 1\n"
      "s_cmp_lg_u32 s36, 0\n"
      "s_cbranch_scc1 LOOP_%=\n"
      "v_cmp_ne_u64 vcc, %[tag], v[56:57]\n"
      "s_mov_b64 s[46:47], %[wo]\n"
      "s_nop 3\n"
      "s_cbranch_vccnz LFLAG_%=\n"
      "LGO_%=:\n"
      "s_waitcnt lgkmcnt(5)\n"
      "v_mfma_f32_32x32x16_bf16 v[176:191], v[24:27], v[16:19], v[176:191]\n"
      "ds_read_b128 v[36:39], %[xa] offset:4096\n"
      "ds_read_b64_tr_b16 v[44:45], %[wa0] offset:3072\n"
      "ds_read_b64_tr_b16 v[46:47], %[wa0] offset:3840\n"
      "ds_read_b64_tr_b16 v[48:49], %[wa1] offset:3072\n"
      "s_waitcnt lgkmcnt(7)\n"
      "v_mfma_f32_32x32x16_bf16 v[192:207], v[28:31], v[16:19], v[192:207]\n"
      "ds_read_b64_tr_b16 v[50:51], %[wa1] offset:3840\n"
      "ds_read_b64_tr_b16 v[52:53], %[wa2] offset:3072\n"
      "ds_read_b64_tr_b16 v[54:55], %[wa2] offset:3840\n"
      "ds_read_b128 v[40:43], %[xa] offset:4608\n"
      "s_add_u32 m0, %[ldx], 24576\n"
      "s_add_u32 s44, %[xb0], s40\n"
      "s_addc_u32 s45, %[xb1], 0\n"
      "global_load_lds_dwordx4 %[xv], s[44:45] sc1\n"
      "s_waitcnt lgkmcnt(9)\n"
      "v_mfma_f32_32x32x16_bf16 v[208:223], v[32:35], v[16:19], v[208:223]\n"
      "global_load_lds_dwordx4 %[xv], s[44:45] offset:1024 sc1\n"
      "s_add_u32 s40, s40, 131072\n"
      "s_waitcnt lgkmcnt(8)\n"
      "v_mfma_f32_32x32x16_bf16 v[60:75], v[32:35], v[20:23], v[60:75]\n"
      "s_add_u32 m0, %[ldw], 18432\n"
      "s_add_u32 s44, %[wb0], s42\n"
      "s_addc_u32 s45, %[wb1], 0\n"
      "global_load_lds_dwordx4 %[wv0], s[44:45]\n"
      "v_mfma_f32_32x32x16_bf16 v[240:255], v[28:31], v[20:23], v[240:255]\n"
      "s_add_u32 m0, m0, 0x400\n"
      "s_add_u32 s42, s42, 98304\n"
      "global_load_lds_dwordx4 %[wv1], s[44:45]\n"
      "v_mfma_f32_32x32x16_bf16 v[224:239], v[24:27], v[20:23], v[224:239]\n"
      "s_add_u32 m0, m0, 0x400\n"
      "s_cmp_eq_u32 s42, 1966080\n"
      "global_load_lds_dwordx4 %[wv2], s[44:45]\n"
      "s_cselect_b32 s40, 0, s40\n"
      "s_cselect_b32 s42, 0, s42\n"
      "s_waitcnt lgkmcnt(5)\n"
      "v_mfma_f32_32x32x16_bf16 v[176:191], v[44:47], v[36:39], v[176:191]\n"
      "ds_read_b128 v[16:19], %[xa] offset:8192\n"
      "ds_read_b64_tr_b16 v[24:25], %[wa0] offset:6144\n"
      "ds_read_b64_tr_b16 v[26:27], %[wa0] offset:6912\n"
      "ds_read_b64_tr_b16 v[28:29], %[wa1] offset:6144\n"
      "s_waitcnt lgkmcnt(7)\n"
      "v_mfma_f32_32x32x16_bf16 v[192:207], v[48:51], v[36:39], v[192:207]\n"
      "ds_read_b64_tr_b16 v[30:31], %[wa1] offset:6912\n"
      "ds_read_b64_tr_b16 v[32:33], %[wa2] offset:6144\n"
      "ds_read_b64_tr_b16 v[34:35], %[wa2] offset:6912\n"
      "ds_read_b128 v[20:23], %[xa] offset:8704\n"
      "global_load_dwordx4 v[116:119], %[xv], s[46:47] offset:-2048 sc1\n"
      "s_waitcnt lgkmcnt(9)\n"
      "v_mfma_f32_32x32x16_bf16 v[208:223], v[52:55], v[36:39], v[208:223]\n"
      "global_load_dwordx4 v[120:123], %[xv], s[46:47] offset:-1024 sc1\n"
      "s_waitcnt lgkmcnt(8)\n"
      "v_mfma_f32_32x32x16_bf16 v[60:75], v[52:55], v[40:43], v[60:75]\n"
      "global_load_dwordx4 v[124:127], %[xv], s[46:47] offset:0 sc1\n"
      "v_mfma_f32_32x32x16_bf16 v[240:255], v[48:51], v[40:43], v[240:255]\n"
      "global_load_dwordx4 v[128:131], %[xv], s[46:47] offset:1024 sc1\n"
      "v_mfma_f32_32x32x16_bf16 v[224:239], v[44:47], v[40:43], v[224:239]\n"
      "global_load_dwordx4 v[132:135], %[xv], s[46:47] offset:2048 sc1\n"
      "s_add_u32 s46, s46, 5120\n"
      "s_addc_u32 s47, s47, 0\n"
      "s_waitcnt lgkmcnt(5)\n"
      "v_mfma_f32_32x32x16_bf16 v[176:191], v[24:27], v[16:19], v[176:191]\n"
      "ds_read_b128 v[36:39], %[xa] offset:12288\n"
      "ds_read_b64_tr_b16 v[44:45], %[wa0] offset:9216\n"
      "ds_read_b64_tr_b16 v[46:47], %[wa0] offset:9984\n"
      "ds_read_b64_tr_b16 v[48:49], %[wa1] offset:9216\n"
      "s_waitcnt lgkmcnt(7)\n"
      "v_mfma_f32_32x32x16_bf16 v[192:207], v[28:31], v[16:19], v[192:207]\n"
      "ds_read_b64_tr_b16 v[50:51], %[wa1] offset:9984\n"
      "ds_read_b64_tr_b16 v[52:53], %[wa2] offset:9216\n"
      "ds_read_b64_tr_b16 v[54:55], %[wa2] offset:9984\n"
      "ds_read_b128 v[40:43], %[xa] offset:12800\n"
      "s_waitcnt lgkmcnt(9)\n"
      "v_mfma_f32_32x32x16_bf16 v[208:223], v[32:35], v[16:19], v[208:223]\n"
      "s_waitcnt lgkmcnt(8)\n"
      "v_mfma_f32_32x32x16_bf16 v[60:75], v[32:35], v[20:23], v[60:75]\n"
      "v_mfma_f32_32x32x16_bf16 v[240:255], v[28:31], v[20:23], v[240:255]\n"
      "v_mfma_f32_32x32x16_bf16 v[224:239], v[24:27], v[20:23], v[224:239]\n"
      "s_waitcnt vmcnt(5) lgkmcnt(0)\n"
      "s_barrier\n"
      "v_mfma_f32_32x32x16_bf16 v[176:191], v[44:47], v[36:39], v[176:191]\n"
      "ds_read_b128 v[16:19], %[xa] offset:16384\n"
      "ds_read_b64_tr_b16 v[24:25], %[wa0] offset:12288\n"
      "ds_read_b64_tr_b16 v[26:27], %[wa0] offset:13056\n"
      "ds_read_b64_tr_b16 v[28:29], %[wa1] offset:12288\n"
      "v_mfma_f32_32x32x16_bf16 v[192:207], v[48:51], v[36:39], v[192:207]\n"
      "ds_read_b64_tr_b16 v[30:31], %[wa1] offset:13056\n"
      "ds_read_b64_tr_b16 v[32:33], %[wa2] offset:12288\n"
      "ds_read_b64_tr_b16 v[34:35], %[wa2] offset:13056\n"
      "ds_read_b128 v[20:23], %[xa] offset:16896\n"
      "global_load_dwordx4 v[136:139], %[xv], s[46:47] offset:-2048 sc1\n"
      "v_mfma_f32_32x32x16_bf16 v[208:223], v[52:55], v[36:39], v[208:223]\n"
      "global_load_dwordx4 v[140:143], %[xv], s[46:47] offset:-1024 sc1\n"
      "v_mfma_f32_32x32x16_bf16 v[60:75], v[52:55], v[40:43], v[60:75]\n"
      "global_load_dwordx4 v[144:147], %[xv], s[46:47] offset:0 sc1\n"
      "v_mfma_f32_32x32x16_bf16 v[240:255], v[48:51], v[40:43], v[240:255]\n"
      "global_load_dwordx4 v[148:151], %[xv], s[46:47] offset:1024 sc1\n"
      "v_mfma_f32_32x32x16_bf16 v[224:239], v[44:47], v[40:43], v[224:239]\n"
      "global_load_dwordx4 v[152:155], %[xv], s[46:47] offset:2048 sc1\n"
      "s_add_u32 s46, s46, 5120\n"
      "s_addc_u32 s47, s47, 0\n"
      "s_waitcnt lgkmcnt(5)\n"
      "v_mfma_f32_32x32x16_bf16 v[176:191], v[24:27], v[16:19], v[176:191]\n"
      "ds_read_b128 v[36:39], %[xa] offset:20480\n"
      "ds_read_b64_tr_b16 v[44:45], %[wa0] offset:15360\n"
      "ds_read_b64_tr_b16 v[46:47], %[wa0] offset:16128\n"
      "ds_read_b64_tr_b16 v[48:49], %[wa1] offset:15360\n"
      "s_waitcnt lgkmcnt(7)\n"
      "v_mfma_f32_32x32x16_bf16 v[192:207], v[28:31], v[16:19], v[192:207]\n"
      "ds_read_b64_tr_b16 v[50:51], %[wa1] offset:16128\n"
      "ds_read_b64_tr_b16 v[52:53], %[wa2] offset:15360\n"
      "ds_read_b64_tr_b16 v[54:55], %[wa2] offset:16128\n"
      "ds_read_b128 v[40:43], %[xa] offset:20992\n"
      "global_load_dwordx4 v[156:159], %[xv], s[46:47] offset:-2048 sc1\n"
      "s_waitcnt lgkmcnt(9)\n"
      "v_mfma_f32_32x32x16_bf16 v[208:223], v[32:35], v[16:19], v[208:223]\n"
      "global_load_dwordx4 v[160:163], %[xv], s[46:47] offset:-1024 sc1\n"
      "s_waitcnt lgkmcnt(8)\n"
      "v_mfma_f32_32x32x16_bf16 v[60:75], v[32:35], v[20:23], v[60:75]\n"
      "global_load_dwordx4 v[164:167], %[xv], s[46:47] offset:0 sc1\n"
      "v_mfma_f32_32x32x16_bf16 v[240:255], v[28:31], v[20:23], v[240:255]\n"
      "global_load_dwordx4 v[168:171], %[xv], s[46:47] offset:1024 sc1\n"
      "v_mfma_f32_32x32x16_bf16 v[224:239], v[24:27], v[20:23], v[224:239]\n"
      "global_load_dwordx4 v[172:175], %[xv], s[46:47] offset:2048 sc1\n"
      "s_add_u32 s46, s46, 5120\n"
      "s_addc_u32 s47, s47, 0\n"
      "s_waitcnt lgkmcnt(5)\n"
      "v_mfma_f32_32x32x16_bf16 v[176:191], v[44:47], v[36:39], v[176:191]\n"
      "ds_read_b128 v[16:19], %[xa] offset:24576\n"
      "ds_read_b64_tr_b16 v[24:25], %[wa0] offset:18432\n"
      "ds_read_b64_tr_b16 v[26:27], %[wa0] offset:19200\n"
      "ds_read_b64_tr_b16 v[28:29], %[wa1] offset:18432\n"
      "s_waitcnt lgkmcnt(7)\n"
      "v_mfma_f32_32x32x16_bf16 v[192:207], v[48:51], v[36:39], v[192:207]\n"
      "ds_read_b64_tr_b16 v[30:31], %[wa1] offset:19200\n"
      "ds_read_b64_tr_b16 v[32:33], %[wa2] offset:18432\n"
      "ds_read_b64_tr_b16 v[34:35], %[wa2] offset:19200\n"
      "ds_read_b128 v[20:23], %[xa] offset:25088\n"
      "s_waitcnt lgkmcnt(9)\n"
      "v_mfma_f32_32x32x16_bf16 v[208:223], v[52:55], v[36:39], v[208:223]\n"
      "s_waitcnt lgkmcnt(8)\n"
      "v_mfma_f32_32x32x16_bf16 v[60:75], v[52:55], v[40:43], v[60:75]\n"
      "v_mfma_f32_32x32x16_bf16 v[240:255], v[48:51], v[40:43], v[240:255]\n"
      "v_mfma_f32_32x32x16_bf16 v[224:239], v[44:47], v[40:43], v[224:239]\n"
      "s_waitcnt lgkmcnt(5)\n"
      "v_mfma_f32_32x32x16_bf16 v[176:191], v[24:27], v[16:19], v[176:191]\n"
      "ds_read_b128 v[36:39], %[xa] offset:28672\n"
      "ds_read_b64_tr_b16 v[44:45], %[wa0] offset:21504\n"
      "ds_read_b64_tr_b16 v[46:47], %[wa0] offset:22272\n"
      "ds_read_b64_tr_b16 v[48:49], %[wa1] offset:21504\n"
      "s_waitcnt lgkmcnt(7)\n"
      "v_mfma_f32_32x32x16_bf16 v[192:207], v[28:31], v[16:19], v[192:207]\n"
      "ds_read_b64_tr_b16 v[50:51], %[wa1] offset:22272\n"
      "ds_read_b64_tr_b16 v[52:53], %[wa2] offset:21504\n"
      "ds_read_b64_tr_b16 v[54:55], %[wa2] offset:22272\n"
      "ds_read_b128 v[40:43], %[xa] offset:29184\n"
      "s_waitcnt lgkmcnt(9)\n"
      "v_mfma_f32_32x32x16_bf16 v[208:223], v[32:35], v[16:19], v[208:223]\n"
      "s_waitcnt lgkmcnt(8)\n"
      "v_mfma_f32_32x32x16_bf16 v[60:75], v[32:35], v[20:23], v[60:75]\n"
      "v_mfma_f32_32x32x16_bf16 v[240:255], v[28:31], v[20:23], v[240:255]\n"
      "v_mfma_f32_32x32x16_bf16 v[224:239], v[24:27], v[20:23], v[224:239]\n"
      "s_waitcnt vmcnt(15) lgkmcnt(0)\n"
      "s_barrier\n"
      "v_mfma_f32_32x32x16_bf16 v[176:191], v[44:47], v[36:39], v[176:191]\n"
      "v_mfma_f32_32x32x16_bf16 v[192:207], v[48:51], v[36:39], v[192:207]\n"
      "v_mfma_f32_32x32x16_bf16 v[208:223], v[52:55], v[36:39], v[208:223]\n"
      "v_mfma_f32_32x32x16_bf16 v[60:75], v[52:55], v[40:43], v[60:75]\n"
      "v_mfma_f32_32x32x16_bf16 v[240:255], v[48:51], v[40:43], v[240:255]\n"
      "v_mfma_f32_32x32x16_bf16 v[224:239], v[44:47], v[40:43], v[224:239]\n"
      "s_branch LEND_%=\n"
      "LFLAG_%=:\n"
      "s_mov_b32 s36, 0x10000\n"
      "LFLAGB_%=:\n"
      "s_sleep 4\n"
      "global_load_dwordx2 v[56:57], %[fz], %[ws] sc1\n"
      "s_sub_u32 s36, s36, 1\n"
      "s_cmp_eq_u32 s36, 0\n"
      "s_cbranch_scc1 LGO_%=\n"
      "s_waitcnt vmcnt(0)\n"
      "v_cmp_ne_u64 vcc, %[tag], v[56:57]\n"
      "s_nop 4\n"
      "s_cbranch_vccnz LFLAGB_%=\n"
      "s_branch LGO_%=\n"
      "LEND_%=:\n"
      "s_nop 11\n"
      : "=&{v[176:191]}"(acc[0][0]), "=&{v[192:207]}"(acc[0][1]), "=&{v[208:223]}"(acc[0][2]), "=&{v[224:239]}"(acc[1][0]), "=&{v[240:255]}"(acc[1][1]), "=&{v[60:75]}"(acc[1][2]), WQ_OPS("=&") GDBG_OP
      : [xa] "v"(l0 + ((lane >> 5) * 128 + 64 * wr + (lane & 31)) * 16), [wa0] "v"(wa[0]), [wa1] "v"(wa[1]), [wa2] "v"(wa[2]), [fz] "v"(WS_QKF + (240 + (lane & 15)) * QS),
        [xv] "v"(lane * 16), [wv0] "v"(wv[0]), [wv1] "v"(wv[1]), [wv2] "v"(wv[2]), [xb0] "s"((unsigned)xb), [xb1] "s"((unsigned)(xb >> 32)), [wb0] "s"((unsigned)wb),
        [wb1] "s"((unsigned)(wb >> 32)), [ldx] "s"(l0 + w * 2048), [ldw] "s"(l0 + 32768 + wr * 24576 + wc * 3072), [ws] "s"(ws), [tag] "s"(tag), [wo] "s"(wo_img), [k0] "s"(k0)
      : "v16", "v17", "v18", "v19", "v20", "v21", "v22", "v23", "v24", "v25", "v26", "v27", "v28", "v29", "v30", "v31", "v32", "v33", "v34", "v35", "v36", "v37", "v38", "v39", "v40", "v41", "v42", "v43", "v44", "v45", "v46", "v47", "v48", "v49", "v50", "v51", "v52", "v53", "v54", "v55", "v56", "v57", "s36", "s40", "s42", "s44", "s45", "s46", "s47", "scc", "memory", "vcc"
  );
  STAMP(ws, 0, 3);
  __builtin_amdgcn_s_setprio(3);
  lane = fresh_lane();
  const int r = lane & 31, h = lane >> 5;
  {
    const u32x4 ones = {~0u, ~0u, ~0u, ~0u};
#pragma unroll
    for (int k = 0; k < 3; ++k)
      if (k * 4 + w < 10) st128(rs, lane * 16, WS_X2 + ((5 * ct + ((k * 4 + w) >> 1)) * S_ + 128 * rt) * 16 + (w & 1) * 1024, ones);
  }
#pragma unroll
  for (int i = 0; i < 2; ++i)
#pragma unroll
    for (int j = 0; j < 3; ++j) {
      u32x4 o;
#pragma unroll
      for (int e = 0; e < 4; ++e) {
        const f32x2 g = {acc[i][j][2 * e], acc[i][j][2 * e + 1]}, u = {acc[i][j][8 + 2 * e], acc[i][j][9 + 2 * e]};
        const f32x2 t = g * -1.4426950408889634f;
        const f32x2 d = f32x2{__builtin_amdgcn_exp2f(t[0]), __builtin_amdgcn_exp2f(t[1])} + 1.0f;
        const f32x2 hv = (g * u) * f32x2{__builtin_amdgcn_rcpf(d[0]), __builtin_amdgcn_rcpf(d[1])};
        o[e] = pk_bf16(hv[0], hv[1]);
      }
      st128(rs, (h * S_ + r) * 16, WS_X2 + ((80 + ct * 12 + 2 * (3 * wc + j)) * S_ + rt * 128 + 64 * wr + 32 * i) * 16, o);
    }
}

DI void pretile(const u16* wo_, const u16* w2, char* ws, LAS char* slab, int ks0, int cp, int lane) {
  const int n = (ks0 & 3) == cp ? 5 : 4;
  const int g = lane >> 4, q = (lane & 15) >> 2, pp = lane & 3;
  const rsrc_t rs = ws_rsrc(ws);
  unsigned wv[5];
#pragma unroll
  for (int i = 0; i < 5; ++i) { const unsigned p = i * 64 + lane, row = p / 20u, pos = p - row * 20u; wv[i] = (row * D_ + pos * 8) * 2; }
  auto fetch = [&](int k) {
    const int ks = k < 4 ? ks0 + 16 * k : 64 + (ks0 >> 2);
    const u16* src = (ks < 48 ? w2 + ks * 32 * D_ : wo_ + (ks - 48) * 32 * D_) + cp * 160;
#pragma unroll
    for (int i = 0; i < 10; ++i) glds16(src + (i / 5) * 16 * D_, wv[i % 5], lds_addr(slab) + (k & 1) * 10240 + i * 1024);
  };
  fetch(0); fetch(1);
#pragma unroll 1
  for (int k = 0; k < n; ++k) {
    if (k == 0 || k == n - 1) WAIT_V(10); else WAIT_V(20);
    const int ks = k < 4 ? ks0 + 16 * k : 64 + (ks0 >> 2);
    const bool isw2 = ks < 48;
    const LAS char* a0 = slab + (k & 1) * 10240 + (isw2 ? 16 * (g >> 1) + 4 * (g & 1) + q : 8 * g + q) * 320 + pp * 8;
    const LAS char* a1 = a0 + (isw2 ? 2560 : 1280);
#pragma unroll
    for (int t = 0; t < 10; ++t)
      st128(rs, lane * 16, WS_WT + (((2 * cp + t / 5) * 68 + ks) * 5 + t % 5) * 1024, __builtin_bit_cast(u32x4, lds_tr2(a0 + 32 * t, a1 + 32 * t)));
    if (k + 2 < n) fetch(k + 2);
  }
}

DI void out_tile(const u16* x, char* ws, u16* out, LAS char* sm, bool bump, int rt, int ct, const char* wo_img, WoRegs& wq, int w, u64 tag) {
  const int tok0 = rt * 64, col0 = ct * 80;
  int lane = fresh_lane();
  const unsigned ring = lds_addr(sm) + w * 20480;
  asm volatile(
      "s_mov_b64 s[40:41], %[wb]\n"
      "s_add_u32 m0, %[ld], 0\n"
      "s_sub_u32 s42, s40, 2048\n"
      "s_subb_u32 s43, s41, 0\n"
      "global_load_lds_dwordx4 %[wv], s[42:43] offset:0 sc1\n"
      "global_load_lds_dwordx4 %[wv], s[42:43] offset:1024 sc1\n"
      "global_load_lds_dwordx4 %[wv], s[42:43] offset:2048 sc1\n"
      "global_load_lds_dwordx4 %[wv], s[42:43] offset:3072 sc1\n"
      "s_add_u32 m0, m0, 2048\n"
      "s_nop 0\n"
      "global_load_lds_dwordx4 %[wv], s[40:41] offset:2048 sc1\n"
      "s_add_u32 s40, s40, 5120\n"
      "s_addc_u32 s41, s41, 0\n"
      "s_add_u32 m0, %[ld], 5120\n"
      "s_sub_u32 s42, s40, 2048\n"
      "s_subb_u32 s43, s41, 0\n"
      "global_load_lds_dwordx4 %[wv], s[42:43] offset:0 sc1\n"
      "global_load_lds_dwordx4 %[wv], s[42:43] offset:1024 sc1\n"
      "global_load_lds_dwordx4 %[wv], s[42:43] offset:2048 sc1\n"
      "global_load_lds_dwordx4 %[wv], s[42:43] offset:3072 sc1\n"
      "s_add_u32 m0, m0, 2048\n"
      "s_nop 0\n"
      "global_load_lds_dwordx4 %[wv], s[40:41] offset:2048 sc1\n"
      "s_add_u32 s40, s40, 5120\n"
      "s_addc_u32 s41, s41, 0\n"
      :: [wv] "v"(lane * 16), [wb] "s"(wo_img + 3 * 5120), [ld] "s"(ring)
      : "s40", "s41", "s42", "s43", "scc", "memory"
  );
  unsigned go[5], lo[5];
#pragma unroll
  for (int i = 0; i < 5; ++i) {
    const unsigned u = w * 64 + lane + 256 * i, tk = u / 20u, c = u - tk * 20u;
    go[i] = tk * (D_ * 2) + c * 8; lo[i] = tk * 320 + ((c ^ ((tk >> 1) & 3)) * 16);
    asm volatile("" : "+v"(go[i]), "+v"(lo[i]));
  }
  await(ws, WS_HF + ((rt >> 1) * 16 + (lane & 15)) * HS, ~0u, tag, w);
  if (bump) {
    await(ws, WS_LNF + lane * 8, WS_LNF + 512 + lane * 8, tag, w); await(ws, WS_LNF + 1024 + lane * 8, WS_LNF + 1536 + lane * 8, tag, w);
    await(ws, WS_QKF + lane * QS, WS_QKF + (64 + lane) * QS, tag, w); await(ws, WS_QKF + (128 + lane) * QS, WS_QKF + (192 + lane) * QS, tag, w);
    if (w == 0 && lane == 0) st_flag(ws, WS_EP, tag);
  }
  STAMP(ws, 0, 5);
  const uintptr_t wimg = (uintptr_t)ws + WS_WT + (ct * 68 + w * 12) * 5120 + 2048, himg = (uintptr_t)ws + WS_X2 + ((80 + w * 48) * S_ + tok0) * 16;
  const uintptr_t oimg = (uintptr_t)ws + WS_X2 + (w * 20 * S_ + tok0) * 16;
  f32x8 res; u32x2 res4;
  lane = fresh_lane();
#define DDBG_OP
  asm volatile(
      "s_mov_b32 s40, 0\n"
      "s_mov_b32 s41, 0\n"
      "s_mov_b32 s36, 5\n"
      "v_mbcnt_lo_u32_b32 v91, -1, 0\n"
      "v_and_b32 v91, 7, v91\n"
      "v_mad_u32_u24 v91, v91, 5, 1\n"
      "v_lshrrev_b32 v91, 1, v91\n"
      "v_lshlrev_b32 v91, 15, v91\n"
      "s_setprio 0\n"
      "s_add_u32 s44, %[wb0], s40\n"
      "s_addc_u32 s45, %[wb1], 0\n"
      "s_add_u32 s46, %[xb0], s41\n"
      "s_addc_u32 s47, %[xb1], 0\n"
      "s_add_u32 s40, s40, 5120\n"
      "s_add_u32 s41, s41, 131072\n"
      "global_load_dwordx4 v[4:7], %[xv], s[46:47] offset:0 sc1\n"
      "global_load_dwordx4 v[8:11], %[xv], s[46:47] offset:256 sc1\n"
      "global_load_dwordx4 v[12:15], %[xv], s[46:47] offset:512 sc1\n"
      "global_load_dwordx4 v[20:23], %[wv], s[44:45] offset:-2048 sc1\n"
      "global_load_dwordx4 v[24:27], %[wv], s[44:45] offset:-1024 sc1\n"
      "global_load_dwordx4 v[28:31], %[wv], s[44:45] offset:0 sc1\n"
      "global_load_dwordx4 v[32:35], %[wv], s[44:45] offset:1024 sc1\n"
      "global_load_dwordx4 v[36:39], %[wv], s[44:45] offset:2048 sc1\n"
      "global_load_dwordx4 v[16:19], %[xv], s[46:47] offset:768 sc1\n"
      "s_add_u32 s44, %[wb0], s40\n"
      "s_addc_u32 s45, %[wb1], 0\n"
      "s_add_u32 s46, %[xb0], s41\n"
      "s_addc_u32 s47, %[xb1], 0\n"
      "s_add_u32 s40, s40, 5120\n"
      "s_add_u32 s41, s41, 131072\n"
      "global_load_dwordx4 v[40:43], %[xv], s[46:47] offset:0 sc1\n"
      "global_load_dwordx4 v[44:47], %[xv], s[46:47] offset:256 sc1\n"
      "global_load_dwordx4 v[48:51], %[xv], s[46:47] offset:512 sc1\n"
      "global_load_dwordx4 v[56:59], %[wv], s[44:45] offset:-2048 sc1\n"
      "global_load_dwordx4 v[60:63], %[wv], s[44:45] offset:-1024 sc1\n"
      "global_load_dwordx4 v[64:67], %[wv], s[44:45] offset:0 sc1\n"
      "global_load_dwordx4 v[68:71], %[wv], s[44:45] offset:1024 sc1\n"
      "global_load_dwordx4 v[72:75], %[wv], s[44:45] offset:2048 sc1\n"
      "global_load_dwordx4 v[52:55], %[xv], s[46:47] offset:768 sc1\n"
      "s_waitcnt vmcnt(9)\n"
      "s_add_u32 s44, %[wb0], s40\n"
      "s_addc_u32 s45, %[wb1], 0\n"
      "s_add_u32 s46, %[xb0], s41\n"
      "s_addc_u32 s47, %[xb1], 0\n"
      "s_add_u32 s40, s40, 5120\n"
      "s_add_u32 s41, s41, 131072\n"
      "v_mfma_f32_16x16x32_bf16 v[176:179], v[20:23], v[4:7], 0\n"
      "v_mfma_f32_16x16x32_bf16 v[180:183], v[24:27], v[4:7], 0\n"
      "v_mfma_f32_16x16x32_bf16 v[184:187], v[28:31], v[4:7], 0\n"
      "v_mfma_f32_16x16x32_bf16 v[188:191], v[32:35], v[4:7], 0\n"
      "v_mfma_f32_16x16x32_bf16 v[192:195], v[36:39], v[4:7], 0\n"
      "v_mfma_f32_16x16x32_bf16 v[196:199], v[20:23], v[8:11], 0\n"
      "v_mfma_f32_16x16x32_bf16 v[200:203], v[24:27], v[8:11], 0\n"
      "global_load_dwordx4 v[4:7], %[xv], s[46:47] offset:0 sc1\n"
      "v_mfma_f32_16x16x32_bf16 v[204:207], v[28:31], v[8:11], 0\n"
      "v_mfma_f32_16x16x32_bf16 v[208:211], v[32:35], v[8:11], 0\n"
      "v_mfma_f32_16x16x32_bf16 v[212:215], v[36:39], v[8:11], 0\n"
      "v_mfma_f32_16x16x32_bf16 v[216:219], v[20:23], v[12:15], 0\n"
      "v_mfma_f32_16x16x32_bf16 v[220:223], v[24:27], v[12:15], 0\n"
      "global_load_dwordx4 v[8:11], %[xv], s[46:47] offset:256 sc1\n"
      "v_mfma_f32_16x16x32_bf16 v[224:227], v[28:31], v[12:15], 0\n"
      "v_mfma_f32_16x16x32_bf16 v[228:231], v[32:35], v[12:15], 0\n"
      "v_mfma_f32_16x16x32_bf16 v[232:235], v[36:39], v[12:15], 0\n"
      "v_mfma_f32_16x16x32_bf16 v[236:239], v[20:23], v[16:19], 0\n"
      "v_mfma_f32_16x16x32_bf16 v[240:243], v[24:27], v[16:19], 0\n"
      "global_load_dwordx4 v[12:15], %[xv], s[46:47] offset:512 sc1\n"
      "global_load_dwordx4 v[20:23], %[wv], s[44:45] offset:-2048 sc1\n"
      "v_mfma_f32_16x16x32_bf16 v[244:247], v[28:31], v[16:19], 0\n"
      "global_load_dwordx4 v[24:27], %[wv], s[44:45] offset:-1024 sc1\n"
      "v_mfma_f32_16x16x32_bf16 v[248:251], v[32:35], v[16:19], 0\n"
      "global_load_dwordx4 v[28:31], %[wv], s[44:45] offset:0 sc1\n"
      "v_mfma_f32_16x16x32_bf16 v[252:255], v[36:39], v[16:19], 0\n"
      "global_load_dwordx4 v[32:35], %[wv], s[44:45] offset:1024 sc1\n"
      "global_load_dwordx4 v[36:39], %[wv], s[44:45] offset:2048 sc1\n"
      "global_load_dwordx4 v[16:19], %[xv], s[46:47] offset:768 sc1\n"
      ".p2align 3\n"
      "LOOP_%=:\n"
      "s_waitcnt vmcnt(9)\n"
      "s_add_u32 s44, %[wb0], s40\n"
      "s_addc_u32 s45, %[wb1], 0\n"
      "s_add_u32 s46, %[xb0], s41\n"
      "s_addc_u32 s47, %[xb1], 0\n"
      "s_add_u32 s40, s40, 5120\n"
      "s_add_u32 s41, s41, 131072\n"
      "v_mfma_f32_16x16x32_bf16 v[176:179], v[56:59], v[40:43], v[176:179]\n"
      "v_mfma_f32_16x16x32_bf16 v[180:183], v[60:63], v[40:43], v[180:183]\n"
      "v_mfma_f32_16x16x32_bf16 v[184:187], v[64:67], v[40:43], v[184:187]\n"
      "v_mfma_f32_16x16x32_bf16 v[188:191], v[68:71], v[40:43], v[188:191]\n"
      "v_mfma_f32_16x16x32_bf16 v[192:195], v[72:75], v[40:43], v[192:195]\n"
      "v_mfma_f32_16x16x32_bf16 v[196:199], v[56:59], v[44:47], v[196:199]\n"
      "v_mfma_f32_16x16x32_bf16 v[200:203], v[60:63], v[44:47], v[200:203]\n"
      "global_load_dwordx4 v[40:43], %[xv], s[46:47] offset:0 sc1\n"
      "v_mfma_f32_16x16x32_bf16 v[204:207], v[64:67], v[44:47], v[204:207]\n"
      "v_mfma_f32_16x16x32_bf16 v[208:211], v[68:71], v[44:47], v[208:211]\n"
      "v_mfma_f32_16x16x32_bf16 v[212:215], v[72:75], v[44:47], v[212:215]\n"
      "v_mfma_f32_16x16x32_bf16 v[216:219], v[56:59], v[48:51], v[216:219]\n"
      "v_mfma_f32_16x16x32_bf16 v[220:223], v[60:63], v[48:51], v[220:223]\n"
      "global_load_dwordx4 v[44:47], %[xv], s[46:47] offset:256 sc1\n"
      "v_mfma_f32_16x16x32_bf16 v[224:227], v[64:67], v[48:51], v[224:227]\n"
      "v_mfma_f32_16x16x32_bf16 v[228:231], v[68:71], v[48:51], v[228:231]\n"
      "v_mfma_f32_16x16x32_bf16 v[232:235], v[72:75], v[48:51], v[232:235]\n"
      "v_mfma_f32_16x16x32_bf16 v[236:239], v[56:59], v[52:55], v[236:239]\n"
      "v_mfma_f32_16x16x32_bf16 v[240:243], v[60:63], v[52:55], v[240:243]\n"
      "global_load_dwordx4 v[48:51], %[xv], s[46:47] offset:512 sc1\n"
      "global_load_dwordx4 v[56:59], %[wv], s[44:45] offset:-2048 sc1\n"
      "v_mfma_f32_16x16x32_bf16 v[244:247], v[64:67], v[52:55], v[244:247]\n"
      "global_load_dwordx4 v[60:63], %[wv], s[44:45] offset:-1024 sc1\n"
      "v_mfma_f32_16x16x32_bf16 v[248:251], v[68:71], v[52:55], v[248:251]\n"
      "global_load_dwordx4 v[64:67], %[wv], s[44:45] offset:0 sc1\n"
      "v_mfma_f32_16x16x32_bf16 v[252:255], v[72:75], v[52:55], v[252:255]\n"
      "global_load_dwordx4 v[68:71], %[wv], s[44:45] offset:1024 sc1\n"
      "global_load_dwordx4 v[72:75], %[wv], s[44:45] offset:2048 sc1\n"
      "global_load_dwordx4 v[52:55], %[xv], s[46:47] offset:768 sc1\n"
      "s_sub_u32 s36, s36, 1\n"
      "s_cmp_eq_u32 s36, 0\n"
      "s_cbranch_scc1 LEND_%=\n"
      "s_waitcnt vmcnt(9)\n"
      "s_add_u32 s44, %[wb0], s40\n"
      "s_addc_u32 s45, %[wb1], 0\n"
      "s_add_u32 s46, %[xb0], s41\n"
      "s_addc_u32 s47, %[xb1], 0\n"
      "s_add_u32 s40, s40, 5120\n"
      "s_add_u32 s41, s41, 131072\n"
      "v_mfma_f32_16x16x32_bf16 v[176:179], v[20:23], v[4:7], v[176:179]\n"
      "v_mfma_f32_16x16x32_bf16 v[180:183], v[24:27], v[4:7], v[180:183]\n"
      "v_mfma_f32_16x16x32_bf16 v[184:187], v[28:31], v[4:7], v[184:187]\n"
      "v_mfma_f32_16x16x32_bf16 v[188:191], v[32:35], v[4:7], v[188:191]\n"
      "v_mfma_f32_16x16x32_bf16 v[192:195], v[36:39], v[4:7], v[192:195]\n"
      "v_mfma_f32_16x16x32_bf16 v[196:199], v[20:23], v[8:11], v[196:199]\n"
      "v_mfma_f32_16x16x32_bf16 v[200:203], v[24:27], v[8:11], v[200:203]\n"
      "global_load_dwordx4 v[4:7], %[xv], s[46:47] offset:0 sc1\n"
      "v_mfma_f32_16x16x32_bf16 v[204:207], v[28:31], v[8:11], v[204:207]\n"
      "v_mfma_f32_16x16x32_bf16 v[208:211], v[32:35], v[8:11], v[208:211]\n"
      "v_mfma_f32_16x16x32_bf16 v[212:215], v[36:39], v[8:11], v[212:215]\n"
      "v_mfma_f32_16x16x32_bf16 v[216:219], v[20:23], v[12:15], v[216:219]\n"
      "v_mfma_f32_16x16x32_bf16 v[220:223], v[24:27], v[12:15], v[220:223]\n"
      "global_load_dwordx4 v[8:11], %[xv], s[46:47] offset:256 sc1\n"
      "v_mfma_f32_16x16x32_bf16 v[224:227], v[28:31], v[12:15], v[224:227]\n"
      "v_mfma_f32_16x16x32_bf16 v[228:231], v[32:35], v[12:15], v[228:231]\n"
      "v_mfma_f32_16x16x32_bf16 v[232:235], v[36:39], v[12:15], v[232:235]\n"
      "v_mfma_f32_16x16x32_bf16 v[236:239], v[20:23], v[16:19], v[236:239]\n"
      "v_mfma_f32_16x16x32_bf16 v[240:243], v[24:27], v[16:19], v[240:243]\n"
      "global_load_dwordx4 v[12:15], %[xv], s[46:47] offset:512 sc1\n"
      "global_load_dwordx4 v[20:23], %[wv], s[44:45] offset:-2048 sc1\n"
      "v_mfma_f32_16x16x32_bf16 v[244:247], v[28:31], v[16:19], v[244:247]\n"
      "global_load_dwordx4 v[24:27], %[wv], s[44:45] offset:-1024 sc1\n"
      "v_mfma_f32_16x16x32_bf16 v[248:251], v[32:35], v[16:19], v[248:251]\n"
      "global_load_dwordx4 v[28:31], %[wv], s[44:45] offset:0 sc1\n"
      "v_mfma_f32_16x16x32_bf16 v[252:255], v[36:39], v[16:19], v[252:255]\n"
      "global_load_dwordx4 v[32:35], %[wv], s[44:45] offset:1024 sc1\n"
      "global_load_dwordx4 v[36:39], %[wv], s[44:45] offset:2048 sc1\n"
      "global_load_dwordx4 v[16:19], %[xv], s[46:47] offset:768 sc1\n"
      "s_branch LOOP_%=\n"
      "LEND_%=:\n"
      "s_mov_b32 s40, %[ob0]\n"
      "s_mov_b32 s41, %[ob1]\n"
      "global_load_dword v92, v91, s[40:41] offset:512 sc1\n"
      "s_waitcnt vmcnt(10)\n"
      "v_mfma_f32_16x16x32_bf16 v[176:179], v[20:23], v[4:7], v[176:179]\n"
      "v_mfma_f32_16x16x32_bf16 v[180:183], v[24:27], v[4:7], v[180:183]\n"
      "v_mfma_f32_16x16x32_bf16 v[184:187], v[28:31], v[4:7], v[184:187]\n"
      "v_mfma_f32_16x16x32_bf16 v[188:191], v[32:35], v[4:7], v[188:191]\n"
      "v_mfma_f32_16x16x32_bf16 v[192:195], v[36:39], v[4:7], v[192:195]\n"
      "v_mfma_f32_16x16x32_bf16 v[196:199], v[20:23], v[8:11], v[196:199]\n"
      "v_mfma_f32_16x16x32_bf16 v[200:203], v[24:27], v[8:11], v[200:203]\n"
      "v_mfma_f32_16x16x32_bf16 v[204:207], v[28:31], v[8:11], v[204:207]\n"
      "v_mfma_f32_16x16x32_bf16 v[208:211], v[32:35], v[8:11], v[208:211]\n"
      "v_mfma_f32_16x16x32_bf16 v[212:215], v[36:39], v[8:11], v[212:215]\n"
      "v_mfma_f32_16x16x32_bf16 v[216:219], v[20:23], v[12:15], v[216:219]\n"
      "v_mfma_f32_16x16x32_bf16 v[220:223], v[24:27], v[12:15], v[220:223]\n"
      "v_mfma_f32_16x16x32_bf16 v[224:227], v[28:31], v[12:15], v[224:227]\n"
      "v_mfma_f32_16x16x32_bf16 v[228:231], v[32:35], v[12:15], v[228:231]\n"
      "v_mfma_f32_16x16x32_bf16 v[232:235], v[36:39], v[12:15], v[232:235]\n"
      "v_mfma_f32_16x16x32_bf16 v[236:239], v[20:23], v[16:19], v[236:239]\n"
      "v_mfma_f32_16x16x32_bf16 v[240:243], v[24:27], v[16:19], v[240:243]\n"
      "v_mfma_f32_16x16x32_bf16 v[244:247], v[28:31], v[16:19], v[244:247]\n"
      "v_mfma_f32_16x16x32_bf16 v[248:251], v[32:35], v[16:19], v[248:251]\n"
      "v_mfma_f32_16x16x32_bf16 v[252:255], v[36:39], v[16:19], v[252:255]\n"
      "s_waitcnt vmcnt(0)\n"
      "v_cmp_eq_u32 vcc, -1, v92\n"
      "s_nop 0\n"
      "s_cmp_lg_u64 vcc, 0\n"
      "s_cselect_b32 s45, 1, 0\n"
      "s_cbranch_scc1 LNOSPEC_%=\n"
      "s_add_u32 s40, %[ob0], 262144\n"
      "s_addc_u32 s41, %[ob1], 0\n"
      "global_load_dwordx4 v[76:79], %[xv], s[40:41] offset:0 sc1\n"
      "global_load_dwordx4 v[80:83], %[xv], s[40:41] offset:256 sc1\n"
      "global_load_dwordx4 v[84:87], %[xv], s[40:41] offset:512 sc1\n"
      "global_load_dwordx4 v[88:91], %[xv], s[40:41] offset:768 sc1\n"
      "s_add_u32 s40, %[ob0], 0\n"
      "s_addc_u32 s41, %[ob1], 0\n"
      "global_load_dwordx4 v[4:7], %[xv], s[40:41] offset:0 sc1\n"
      "global_load_dwordx4 v[8:11], %[xv], s[40:41] offset:256 sc1\n"
      "global_load_dwordx4 v[12:15], %[xv], s[40:41] offset:512 sc1\n"
      "global_load_dwordx4 v[16:19], %[xv], s[40:41] offset:768 sc1\n"
      "s_add_u32 s40, %[ob0], 131072\n"
      "s_addc_u32 s41, %[ob1], 0\n"
      "global_load_dwordx4 v[20:23], %[xv], s[40:41] offset:0 sc1\n"
      "global_load_dwordx4 v[24:27], %[xv], s[40:41] offset:256 sc1\n"
      "global_load_dwordx4 v[28:31], %[xv], s[40:41] offset:512 sc1\n"
      "global_load_dwordx4 v[32:35], %[xv], s[40:41] offset:768 sc1\n"
      "LNOSPEC_%=:\n"
      "s_waitcnt vmcnt(12)\n"
      "v_mfma_f32_16x16x32_bf16 v[176:179], v[56:59], v[40:43], v[176:179]\n"
      "v_mfma_f32_16x16x32_bf16 v[180:183], v[60:63], v[40:43], v[180:183]\n"
      "v_mfma_f32_16x16x32_bf16 v[184:187], v[64:67], v[40:43], v[184:187]\n"
      "v_mfma_f32_16x16x32_bf16 v[188:191], v[68:71], v[40:43], v[188:191]\n"
      "v_mfma_f32_16x16x32_bf16 v[192:195], v[72:75], v[40:43], v[192:195]\n"
      "v_mfma_f32_16x16x32_bf16 v[196:199], v[56:59], v[44:47], v[196:199]\n"
      "v_mfma_f32_16x16x32_bf16 v[200:203], v[60:63], v[44:47], v[200:203]\n"
      "v_mfma_f32_16x16x32_bf16 v[204:207], v[64:67], v[44:47], v[204:207]\n"
      "v_mfma_f32_16x16x32_bf16 v[208:211], v[68:71], v[44:47], v[208:211]\n"
      "v_mfma_f32_16x16x32_bf16 v[212:215], v[72:75], v[44:47], v[212:215]\n"
      "v_mfma_f32_16x16x32_bf16 v[216:219], v[56:59], v[48:51], v[216:219]\n"
      "v_mfma_f32_16x16x32_bf16 v[220:223], v[60:63], v[48:51], v[220:223]\n"
      "v_mfma_f32_16x16x32_bf16 v[224:227], v[64:67], v[48:51], v[224:227]\n"
      "v_mfma_f32_16x16x32_bf16 v[228:231], v[68:71], v[48:51], v[228:231]\n"
      "v_mfma_f32_16x16x32_bf16 v[232:235], v[72:75], v[48:51], v[232:235]\n"
      "v_mfma_f32_16x16x32_bf16 v[236:239], v[56:59], v[52:55], v[236:239]\n"
      "v_mfma_f32_16x16x32_bf16 v[240:243], v[60:63], v[52:55], v[240:243]\n"
      "v_mfma_f32_16x16x32_bf16 v[244:247], v[64:67], v[52:55], v[244:247]\n"
      "v_mfma_f32_16x16x32_bf16 v[248:251], v[68:71], v[52:55], v[248:251]\n"
      "v_mfma_f32_16x16x32_bf16 v[252:255], v[72:75], v[52:55], v[252:255]\n"
      "s_cmp_lg_u32 s45, 0\n"
      "s_cbranch_scc1 LNOSPEC2_%=\n"
      "s_add_u32 s40, %[ob0], 393216\n"
      "s_addc_u32 s41, %[ob1], 0\n"
      "global_load_dwordx4 v[40:43], %[xv], s[40:41] offset:0 sc1\n"
      "global_load_dwordx4 v[44:47], %[xv], s[40:41] offset:256 sc1\n"
      "global_load_dwordx4 v[48:51], %[xv], s[40:41] offset:512 sc1\n"
      "global_load_dwordx4 v[52:55], %[xv], s[40:41] offset:768 sc1\n"
      "s_add_u32 s40, %[ob0], 524288\n"
      "s_addc_u32 s41, %[ob1], 0\n"
      "global_load_dwordx4 v[56:59], %[xv], s[40:41] offset:0 sc1\n"
      "global_load_dwordx4 v[60:63], %[xv], s[40:41] offset:256 sc1\n"
      "global_load_dwordx4 v[64:67], %[xv], s[40:41] offset:512 sc1\n"
      "global_load_dwordx4 v[68:71], %[xv], s[40:41] offset:768 sc1\n"
      "LNOSPEC2_%=:\n"
      "global_load_dwordx2 v[96:97], %[go0], %[xr]\n"
      "global_load_dwordx2 v[98:99], %[go1], %[xr]\n"
      "global_load_dwordx2 v[100:101], %[go2], %[xr]\n"
      "global_load_dwordx2 v[102:103], %[go3], %[xr]\n"
      "global_load_dwordx2 v[104:105], %[go4], %[xr]\n"
      "v_mbcnt_lo_u32_b32 v92, -1, 0\n"
      "v_mbcnt_hi_u32_b32 v92, -1, v92\n"
      "v_bfe_u32 v93, v92, 1, 2\n"
      "v_lshrrev_b32 v106, 4, v92\n"
      "v_xor_b32 v106, v106, v93\n"
      "v_and_b32 v93, 15, v92\n"
      "v_mul_u32_u24 v93, 320, v93\n"
      "v_lshl_add_u32 v106, v106, 4, v93\n"
      "v_add_u32 v106, %[ld], v106\n"
      "s_mov_b32 s44, 0x10000\n"
      "s_mov_b32 s46, 0\n"
      "s_cmp_lg_u32 s45, 0\n"
      "s_cbranch_scc1 LWATCH0_%=\n"
      "s_waitcnt vmcnt(13)\n"
      "LCHECK0_%=:\n"
      "v_max3_u32 v92, v76, v77, v78\n"
      "v_max3_u32 v93, v79, v80, v81\n"
      "v_max3_u32 v92, v92, v82, v83\n"
      "v_max3_u32 v93, v93, v84, v85\n"
      "v_max3_u32 v92, v92, v86, v87\n"
      "v_max3_u32 v93, v93, v88, v89\n"
      "v_max3_u32 v92, v92, v90, v91\n"
      "v_max3_u32 v93, v93, v4, v5\n"
      "v_max3_u32 v92, v92, v6, v7\n"
      "v_max3_u32 v93, v93, v8, v9\n"
      "v_max3_u32 v92, v92, v10, v11\n"
      "v_max3_u32 v93, v93, v12, v13\n"
      "v_max3_u32 v92, v92, v14, v15\n"
      "v_max3_u32 v93, v93, v16, v17\n"
      "v_max3_u32 v92, v92, v18, v19\n"
      "v_max3_u32 v93, v93, v20, v21\n"
      "v_max3_u32 v92, v92, v22, v23\n"
      "v_max3_u32 v93, v93, v24, v25\n"
      "v_max3_u32 v92, v92, v26, v27\n"
      "v_max3_u32 v93, v93, v28, v29\n"
      "v_max3_u32 v92, v92, v30, v31\n"
      "v_max3_u32 v93, v93, v32, v33\n"
      "v_max3_u32 v92, v92, v34, v35\n"
      "v_max_u32 v92, v92, v93\n"
      "v_cmp_eq_u32 vcc, -1, v92\n"
      "s_cbranch_vccz LDONE0_%=\n"
      "s_cmp_lg_u32 s46, 0\n"
      "s_cbranch_scc1 LDONE0_%=\n"
      "LWATCH0_%=:\n"
      "s_waitcnt vmcnt(0)\n"
      "s_mov_b32 s40, %[ob0]\n"
      "s_mov_b32 s41, %[ob1]\n"
      "s_mov_b64 exec, 0xff\n"
      "v_mbcnt_lo_u32_b32 v92, -1, 0\n"
      "v_lshl_add_u32 v77, v92, 7, %[fo]\n"
      "v_mad_u32_u24 v76, v92, 5, 1\n"
      "v_lshrrev_b32 v76, 1, v76\n"
      "v_lshlrev_b32 v76, 15, v76\n"
      "LGATE0_%=:\n"
      "global_load_dword v92, v76, s[40:41] offset:512 sc1\n"
      "global_load_dwordx2 v[94:95], v77, %[ws] sc1\n"
      "s_waitcnt vmcnt(0)\n"
      "v_cmp_ne_u32 vcc, -1, v92\n"
      "v_cmp_eq_u64 s[42:43], %[tag], v[94:95]\n"
      "s_sub_u32 s44, s44, 1\n"
      "s_cselect_b32 s46, 1, 0\n"
      "s_cmp_eq_u32 s42, 0xff\n"
      "s_cselect_b32 s46, 1, s46\n"
      "s_or_b32 s45, vcc_lo, s42\n"
      "s_cmp_lg_u32 s46, 0\n"
      "s_cselect_b32 s45, 0xff, s45\n"
      "s_cmp_eq_u32 s45, 0xff\n"
      "s_cbranch_scc1 LOPEN0_%=\n"
      "s_sleep 2\n"
      "s_branch LGATE0_%=\n"
      "LOPEN0_%=:\n"
      "s_mov_b64 exec, -1\n"
      "s_add_u32 s40, %[ob0], 262144\n"
      "s_addc_u32 s41, %[ob1], 0\n"
      "global_load_dwordx4 v[76:79], %[xv], s[40:41] offset:0 sc1\n"
      "global_load_dwordx4 v[80:83], %[xv], s[40:41] offset:256 sc1\n"
      "global_load_dwordx4 v[84:87], %[xv], s[40:41] offset:512 sc1\n"
      "global_load_dwordx4 v[88:91], %[xv], s[40:41] offset:768 sc1\n"
      "s_add_u32 s40, %[ob0], 0\n"
      "s_addc_u32 s41, %[ob1], 0\n"
      "global_load_dwordx4 v[4:7], %[xv], s[40:41] offset:0 sc1\n"
      "global_load_dwordx4 v[8:11], %[xv], s[40:41] offset:256 sc1\n"
      "global_load_dwordx4 v[12:15], %[xv], s[40:41] offset:512 sc1\n"
      "global_load_dwordx4 v[16:19], %[xv], s[40:41] offset:768 sc1\n"
      "s_add_u32 s40, %[ob0], 131072\n"
      "s_addc_u32 s41, %[ob1], 0\n"
      "global_load_dwordx4 v[20:23], %[xv], s[40:41] offset:0 sc1\n"
      "global_load_dwordx4 v[24:27], %[xv], s[40:41] offset:256 sc1\n"
      "global_load_dwordx4 v[28:31], %[xv], s[40:41] offset:512 sc1\n"
      "global_load_dwordx4 v[32:35], %[xv], s[40:41] offset:768 sc1\n"
      "s_add_u32 s40, %[ob0], 393216\n"
      "s_addc_u32 s41, %[ob1], 0\n"
      "global_load_dwordx4 v[40:43], %[xv], s[40:41] offset:0 sc1\n"
      "global_load_dwordx4 v[44:47], %[xv], s[40:41] offset:256 sc1\n"
      "global_load_dwordx4 v[48:51], %[xv], s[40:41] offset:512 sc1\n"
      "global_load_dwordx4 v[52:55], %[xv], s[40:41] offset:768 sc1\n"
      "s_add_u32 s40, %[ob0], 524288\n"
      "s_addc_u32 s41, %[ob1], 0\n"
      "global_load_dwordx4 v[56:59], %[xv], s[40:41] offset:0 sc1\n"
      "global_load_dwordx4 v[60:63], %[xv], s[40:41] offset:256 sc1\n"
      "global_load_dwordx4 v[64:67], %[xv], s[40:41] offset:512 sc1\n"
      "global_load_dwordx4 v[68:71], %[xv], s[40:41] offset:768 sc1\n"
      "s_waitcnt vmcnt(8)\n"
      "s_branch LCHECK0_%=\n"
      "LDONE0_%=:\n"
      "v_add_u32 v92, %[ld], %[wv]\n"
      "v_mfma_f32_16x16x32_bf16 v[176:179], v[116:119], v[4:7], v[176:179]\n"
      "v_mfma_f32_16x16x32_bf16 v[180:183], v[120:123], v[4:7], v[180:183]\n"
      "v_mfma_f32_16x16x32_bf16 v[184:187], v[124:127], v[4:7], v[184:187]\n"
      "v_mfma_f32_16x16x32_bf16 v[188:191], v[128:131], v[4:7], v[188:191]\n"
      "v_mfma_f32_16x16x32_bf16 v[192:195], v[132:135], v[4:7], v[192:195]\n"
      "v_mfma_f32_16x16x32_bf16 v[196:199], v[116:119], v[8:11], v[196:199]\n"
      "v_mfma_f32_16x16x32_bf16 v[200:203], v[120:123], v[8:11], v[200:203]\n"
      "v_mfma_f32_16x16x32_bf16 v[204:207], v[124:127], v[8:11], v[204:207]\n"
      "v_mfma_f32_16x16x32_bf16 v[208:211], v[128:131], v[8:11], v[208:211]\n"
      "v_mfma_f32_16x16x32_bf16 v[212:215], v[132:135], v[8:11], v[212:215]\n"
      "v_mfma_f32_16x16x32_bf16 v[216:219], v[116:119], v[12:15], v[216:219]\n"
      "v_mfma_f32_16x16x32_bf16 v[220:223], v[120:123], v[12:15], v[220:223]\n"
      "v_mfma_f32_16x16x32_bf16 v[224:227], v[124:127], v[12:15], v[224:227]\n"
      "v_mfma_f32_16x16x32_bf16 v[228:231], v[128:131], v[12:15], v[228:231]\n"
      "v_mfma_f32_16x16x32_bf16 v[232:235], v[132:135], v[12:15], v[232:235]\n"
      "v_mfma_f32_16x16x32_bf16 v[236:239], v[116:119], v[16:19], v[236:239]\n"
      "ds_read_b128 v[116:119], v92 offset:0\n"
      "v_mfma_f32_16x16x32_bf16 v[240:243], v[120:123], v[16:19], v[240:243]\n"
      "ds_read_b128 v[120:123], v92 offset:1024\n"
      "v_mfma_f32_16x16x32_bf16 v[244:247], v[124:127], v[16:19], v[244:247]\n"
      "ds_read_b128 v[124:127], v92 offset:2048\n"
      "v_mfma_f32_16x16x32_bf16 v[248:251], v[128:131], v[16:19], v[248:251]\n"
      "ds_read_b128 v[128:131], v92 offset:3072\n"
      "v_mfma_f32_16x16x32_bf16 v[252:255], v[132:135], v[16:19], v[252:255]\n"
      "ds_read_b128 v[132:135], v92 offset:4096\n"
      "v_mfma_f32_16x16x32_bf16 v[176:179], v[136:139], v[20:23], v[176:179]\n"
      "v_mfma_f32_16x16x32_bf16 v[180:183], v[140:143], v[20:23], v[180:183]\n"
      "v_mfma_f32_16x16x32_bf16 v[184:187], v[144:147], v[20:23], v[184:187]\n"
      "v_mfma_f32_16x16x32_bf16 v[188:191], v[148:151], v[20:23], v[188:191]\n"
      "v_mfma_f32_16x16x32_bf16 v[192:195], v[152:155], v[20:23], v[192:195]\n"
      "v_mfma_f32_16x16x32_bf16 v[196:199], v[136:139], v[24:27], v[196:199]\n"
      "v_mfma_f32_16x16x32_bf16 v[200:203], v[140:143], v[24:27], v[200:203]\n"
      "v_mfma_f32_16x16x32_bf16 v[204:207], v[144:147], v[24:27], v[204:207]\n"
      "v_mfma_f32_16x16x32_bf16 v[208:211], v[148:151], v[24:27], v[208:211]\n"
      "v_mfma_f32_16x16x32_bf16 v[212:215], v[152:155], v[24:27], v[212:215]\n"
      "v_mfma_f32_16x16x32_bf16 v[216:219], v[136:139], v[28:31], v[216:219]\n"
      "v_mfma_f32_16x16x32_bf16 v[220:223], v[140:143], v[28:31], v[220:223]\n"
      "v_mfma_f32_16x16x32_bf16 v[224:227], v[144:147], v[28:31], v[224:227]\n"
      "v_mfma_f32_16x16x32_bf16 v[228:231], v[148:151], v[28:31], v[228:231]\n"
      "v_mfma_f32_16x16x32_bf16 v[232:235], v[152:155], v[28:31], v[232:235]\n"
      "v_mfma_f32_16x16x32_bf16 v[236:239], v[136:139], v[32:35], v[236:239]\n"
      "ds_read_b128 v[136:139], v92 offset:5120\n"
      "v_mfma_f32_16x16x32_bf16 v[240:243], v[140:143], v[32:35], v[240:243]\n"
      "ds_read_b128 v[140:143], v92 offset:6144\n"
      "v_mfma_f32_16x16x32_bf16 v[244:247], v[144:147], v[32:35], v[244:247]\n"
      "ds_read_b128 v[144:147], v92 offset:7168\n"
      "v_mfma_f32_16x16x32_bf16 v[248:251], v[148:151], v[32:35], v[248:251]\n"
      "ds_read_b128 v[148:151], v92 offset:8192\n"
      "v_mfma_f32_16x16x32_bf16 v[252:255], v[152:155], v[32:35], v[252:255]\n"
      "ds_read_b128 v[152:155], v92 offset:9216\n"
      "v_mfma_f32_16x16x32_bf16 v[176:179], v[156:159], v[76:79], v[176:179]\n"
      "v_mfma_f32_16x16x32_bf16 v[180:183], v[160:163], v[76:79], v[180:183]\n"
      "v_mfma_f32_16x16x32_bf16 v[184:187], v[164:167], v[76:79], v[184:187]\n"
      "v_mfma_f32_16x16x32_bf16 v[188:191], v[168:171], v[76:79], v[188:191]\n"
      "v_mfma_f32_16x16x32_bf16 v[192:195], v[172:175], v[76:79], v[192:195]\n"
      "v_mfma_f32_16x16x32_bf16 v[196:199], v[156:159], v[80:83], v[196:199]\n"
      "v_mfma_f32_16x16x32_bf16 v[200:203], v[160:163], v[80:83], v[200:203]\n"
      "v_mfma_f32_16x16x32_bf16 v[204:207], v[164:167], v[80:83], v[204:207]\n"
      "v_mfma_f32_16x16x32_bf16 v[208:211], v[168:171], v[80:83], v[208:211]\n"
      "v_mfma_f32_16x16x32_bf16 v[212:215], v[172:175], v[80:83], v[212:215]\n"
      "s_waitcnt vmcnt(0)\n"
      "LCHECK1_%=:\n"
      "v_max3_u32 v92, v40, v41, v42\n"
      "v_max3_u32 v93, v43, v44, v45\n"
      "v_max3_u32 v92, v92, v46, v47\n"
      "v_max3_u32 v93, v93, v48, v49\n"
      "v_max3_u32 v92, v92, v50, v51\n"
      "v_max3_u32 v93, v93, v52, v53\n"
      "v_max3_u32 v92, v92, v54, v55\n"
      "v_max3_u32 v93, v93, v56, v57\n"
      "v_max3_u32 v92, v92, v58, v59\n"
      "v_max3_u32 v93, v93, v60, v61\n"
      "v_max3_u32 v92, v92, v62, v63\n"
      "v_max3_u32 v93, v93, v64, v65\n"
      "v_max3_u32 v92, v92, v66, v67\n"
      "v_max3_u32 v93, v93, v68, v69\n"
      "v_max3_u32 v92, v92, v70, v71\n"
      "v_max_u32 v92, v92, v93\n"
      "v_cmp_eq_u32 vcc, -1, v92\n"
      "s_cbranch_vccz LDONE1_%=\n"
      "s_cmp_lg_u32 s46, 0\n"
      "s_cbranch_scc1 LDONE1_%=\n"
      "LWATCH1_%=:\n"
      "s_waitcnt vmcnt(0)\n"
      "s_mov_b32 s40, %[ob0]\n"
      "s_mov_b32 s41, %[ob1]\n"
      "s_mov_b64 exec, 0xff\n"
      "v_mbcnt_lo_u32_b32 v92, -1, 0\n"
      "v_lshl_add_u32 v41, v92, 7, %[fo]\n"
      "v_mad_u32_u24 v40, v92, 5, 1\n"
      "v_lshrrev_b32 v40, 1, v40\n"
      "v_lshlrev_b32 v40, 15, v40\n"
      "LGATE1_%=:\n"
      "global_load_dword v92, v40, s[40:41] offset:512 sc1\n"
      "global_load_dwordx2 v[94:95], v41, %[ws] sc1\n"
      "s_waitcnt vmcnt(0)\n"
      "v_cmp_ne_u32 vcc, -1, v92\n"
      "v_cmp_eq_u64 s[42:43], %[tag], v[94:95]\n"
      "s_sub_u32 s44, s44, 1\n"
      "s_cselect_b32 s46, 1, 0\n"
      "s_cmp_eq_u32 s42, 0xff\n"
      "s_cselect_b32 s46, 1, s46\n"
      "s_or_b32 s45, vcc_lo, s42\n"
      "s_cmp_lg_u32 s46, 0\n"
      "s_cselect_b32 s45, 0xff, s45\n"
      "s_cmp_eq_u32 s45, 0xff\n"
      "s_cbranch_scc1 LOPEN1_%=\n"
      "s_sleep 2\n"
      "s_branch LGATE1_%=\n"
      "LOPEN1_%=:\n"
      "s_mov_b64 exec, -1\n"
      "s_add_u32 s40, %[ob0], 393216\n"
      "s_addc_u32 s41, %[ob1], 0\n"
      "global_load_dwordx4 v[40:43], %[xv], s[40:41] offset:0 sc1\n"
      "global_load_dwordx4 v[44:47], %[xv], s[40:41] offset:256 sc1\n"
      "global_load_dwordx4 v[48:51], %[xv], s[40:41] offset:512 sc1\n"
      "global_load_dwordx4 v[52:55], %[xv], s[40:41] offset:768 sc1\n"
      "s_add_u32 s40, %[ob0], 524288\n"
      "s_addc_u32 s41, %[ob1], 0\n"
      "global_load_dwordx4 v[56:59], %[xv], s[40:41] offset:0 sc1\n"
      "global_load_dwordx4 v[60:63], %[xv], s[40:41] offset:256 sc1\n"
      "global_load_dwordx4 v[64:67], %[xv], s[40:41] offset:512 sc1\n"
      "global_load_dwordx4 v[68:71], %[xv], s[40:41] offset:768 sc1\n"
      "s_waitcnt vmcnt(0)\n"
      "s_branch LCHECK1_%=\n"
      "LDONE1_%=:\n"
      "v_mfma_f32_16x16x32_bf16 v[216:219], v[156:159], v[84:87], v[216:219]\n"
      "v_mfma_f32_16x16x32_bf16 v[220:223], v[160:163], v[84:87], v[220:223]\n"
      "v_mfma_f32_16x16x32_bf16 v[224:227], v[164:167], v[84:87], v[224:227]\n"
      "v_mfma_f32_16x16x32_bf16 v[228:231], v[168:171], v[84:87], v[228:231]\n"
      "v_mfma_f32_16x16x32_bf16 v[232:235], v[172:175], v[84:87], v[232:235]\n"
      "v_mfma_f32_16x16x32_bf16 v[236:239], v[156:159], v[88:91], v[236:239]\n"
      "v_mfma_f32_16x16x32_bf16 v[240:243], v[160:163], v[88:91], v[240:243]\n"
      "v_mfma_f32_16x16x32_bf16 v[244:247], v[164:167], v[88:91], v[244:247]\n"
      "v_mfma_f32_16x16x32_bf16 v[248:251], v[168:171], v[88:91], v[248:251]\n"
      "v_mfma_f32_16x16x32_bf16 v[252:255], v[172:175], v[88:91], v[252:255]\n"
      "s_waitcnt vmcnt(0) lgkmcnt(0)\n"
      "v_mfma_f32_16x16x32_bf16 v[176:179], v[116:119], v[40:43], v[176:179]\n"
      "v_mfma_f32_16x16x32_bf16 v[180:183], v[120:123], v[40:43], v[180:183]\n"
      "v_mfma_f32_16x16x32_bf16 v[184:187], v[124:127], v[40:43], v[184:187]\n"
      "v_mfma_f32_16x16x32_bf16 v[188:191], v[128:131], v[40:43], v[188:191]\n"
      "v_mfma_f32_16x16x32_bf16 v[192:195], v[132:135], v[40:43], v[192:195]\n"
      "v_mfma_f32_16x16x32_bf16 v[176:179], v[136:139], v[56:59], v[176:179]\n"
      "v_mfma_f32_16x16x32_bf16 v[180:183], v[140:143], v[56:59], v[180:183]\n"
      "v_mfma_f32_16x16x32_bf16 v[184:187], v[144:147], v[56:59], v[184:187]\n"
      "v_mfma_f32_16x16x32_bf16 v[188:191], v[148:151], v[56:59], v[188:191]\n"
      "v_mfma_f32_16x16x32_bf16 v[192:195], v[152:155], v[56:59], v[192:195]\n"
      "v_mfma_f32_16x16x32_bf16 v[196:199], v[116:119], v[44:47], v[196:199]\n"
      "v_mfma_f32_16x16x32_bf16 v[200:203], v[120:123], v[44:47], v[200:203]\n"
      "v_mfma_f32_16x16x32_bf16 v[204:207], v[124:127], v[44:47], v[204:207]\n"
      "v_mfma_f32_16x16x32_bf16 v[208:211], v[128:131], v[44:47], v[208:211]\n"
      "v_mfma_f32_16x16x32_bf16 v[212:215], v[132:135], v[44:47], v[212:215]\n"
      "v_mfma_f32_16x16x32_bf16 v[196:199], v[136:139], v[60:63], v[196:199]\n"
      "ds_write_b128 v106, v[176:179] offset:0\n"
      "v_mfma_f32_16x16x32_bf16 v[200:203], v[140:143], v[60:63], v[200:203]\n"
      "ds_write_b128 v106, v[180:183] offset:64\n"
      "v_mfma_f32_16x16x32_bf16 v[204:207], v[144:147], v[60:63], v[204:207]\n"
      "ds_write_b128 v106, v[184:187] offset:128\n"
      "v_mfma_f32_16x16x32_bf16 v[208:211], v[148:151], v[60:63], v[208:211]\n"
      "ds_write_b128 v106, v[188:191] offset:192\n"
      "v_mfma_f32_16x16x32_bf16 v[212:215], v[152:155], v[60:63], v[212:215]\n"
      "ds_write_b128 v106, v[192:195] offset:256\n"
      "v_mfma_f32_16x16x32_bf16 v[216:219], v[116:119], v[48:51], v[216:219]\n"
      "v_mfma_f32_16x16x32_bf16 v[220:223], v[120:123], v[48:51], v[220:223]\n"
      "v_mfma_f32_16x16x32_bf16 v[224:227], v[124:127], v[48:51], v[224:227]\n"
      "v_mfma_f32_16x16x32_bf16 v[228:231], v[128:131], v[48:51], v[228:231]\n"
      "v_mfma_f32_16x16x32_bf16 v[232:235], v[132:135], v[48:51], v[232:235]\n"
      "v_mfma_f32_16x16x32_bf16 v[216:219], v[136:139], v[64:67], v[216:219]\n"
      "ds_write_b128 v106, v[196:199] offset:5120\n"
      "v_mfma_f32_16x16x32_bf16 v[220:223], v[140:143], v[64:67], v[220:223]\n"
      "ds_write_b128 v106, v[200:203] offset:5184\n"
      "v_mfma_f32_16x16x32_bf16 v[224:227], v[144:147], v[64:67], v[224:227]\n"
      "ds_write_b128 v106, v[204:207] offset:5248\n"
      "v_mfma_f32_16x16x32_bf16 v[228:231], v[148:151], v[64:67], v[228:231]\n"
      "ds_write_b128 v106, v[208:211] offset:5312\n"
      "v_mfma_f32_16x16x32_bf16 v[232:235], v[152:155], v[64:67], v[232:235]\n"
      "ds_write_b128 v106, v[212:215] offset:5376\n"
      "v_mfma_f32_16x16x32_bf16 v[236:239], v[116:119], v[52:55], v[236:239]\n"
      "v_mfma_f32_16x16x32_bf16 v[240:243], v[120:123], v[52:55], v[240:243]\n"
      "v_mfma_f32_16x16x32_bf16 v[244:247], v[124:127], v[52:55], v[244:247]\n"
      "v_mfma_f32_16x16x32_bf16 v[248:251], v[128:131], v[52:55], v[248:251]\n"
      "v_mfma_f32_16x16x32_bf16 v[252:255], v[132:135], v[52:55], v[252:255]\n"
      "v_mfma_f32_16x16x32_bf16 v[236:239], v[136:139], v[68:71], v[236:239]\n"
      "ds_write_b128 v106, v[216:219] offset:10240\n"
      "v_mfma_f32_16x16x32_bf16 v[240:243], v[140:143], v[68:71], v[240:243]\n"
      "ds_write_b128 v106, v[220:223] offset:10304\n"
      "v_mfma_f32_16x16x32_bf16 v[244:247], v[144:147], v[68:71], v[244:247]\n"
      "ds_write_b128 v106, v[224:227] offset:10368\n"
      "v_mfma_f32_16x16x32_bf16 v[248:251], v[148:151], v[68:71], v[248:251]\n"
      "ds_write_b128 v106, v[228:231] offset:10432\n"
      "v_mfma_f32_16x16x32_bf16 v[252:255], v[152:155], v[68:71], v[252:255]\n"
      "ds_write_b128 v106, v[232:235] offset:10496\n"
      "s_nop 3\n"
      "ds_write_b128 v106, v[236:239] offset:15360\n"
      "s_nop 0\n"
      "ds_write_b128 v106, v[240:243] offset:15424\n"
      "s_nop 0\n"
      "ds_write_b128 v106, v[244:247] offset:15488\n"
      "s_nop 0\n"
      "ds_write_b128 v106, v[248:251] offset:15552\n"
      "s_nop 0\n"
      "ds_write_b128 v106, v[252:255] offset:15616\n"
      "s_waitcnt lgkmcnt(0)\n"
      "s_barrier\n"
      : WQ_OPS("+"), "=&{v[96:103]}"(res), "=&{v[104:105]}"(res4) DDBG_OP
      : [wv] "v"(lane * 16), [xv] "v"(((lane >> 4) * S_ + (lane & 15)) * 16), [go0] "v"(go[0]), [go1] "v"(go[1]), [go2] "v"(go[2]), [go3] "v"(go[3]), [go4] "v"(go[4]),
        [wb0] "s"((unsigned)wimg), [wb1] "s"((unsigned)(wimg >> 32)), [xb0] "s"((unsigned)himg), [xb1] "s"((unsigned)(himg >> 32)),
        [ob0] "s"((unsigned)oimg), [ob1] "s"((unsigned)(oimg >> 32)), [xr] "s"(x + tok0 * D_ + col0), [fo] "s"(WS_OF + ((rt >> 2) * 32 + 8 * w) * OS), [ld] "s"(ring),
        [ws] "s"(ws), [tag] "s"(tag)
      : "v4", "v5", "v6", "v7", "v8", "v9", "v10", "v11", "v12", "v13", "v14", "v15", "v16", "v17", "v18", "v19", "v20", "v21", "v22", "v23", "v24", "v25", "v26", "v27", "v28", "v29", "v30", "v31", "v32", "v33", "v34", "v35", "v36", "v37", "v38", "v39", "v40", "v41", "v42", "v43", "v44", "v45", "v46", "v47", "v48", "v49", "v50", "v51", "v52", "v53", "v54", "v55", "v56", "v57", "v58", "v59", "v60", "v61", "v62", "v63", "v64", "v65", "v66", "v67", "v68", "v69", "v70", "v71", "v72", "v73", "v74", "v75", "v76", "v77", "v78", "v79", "v80", "v81", "v82", "v83", "v84", "v85", "v86", "v87", "v88", "v89", "v90", "v91", "v92", "v93", "v94", "v95", "v106", "v176", "v177", "v178", "v179", "v180", "v181", "v182", "v183", "v184", "v185", "v186", "v187", "v188", "v189", "v190", "v191", "v192", "v193", "v194", "v195", "v196", "v197", "v198", "v199", "v200", "v201", "v202", "v203", "v204", "v205", "v206", "v207", "v208", "v209", "v210", "v211", "v212", "v213", "v214", "v215", "v216", "v217", "v218", "v219", "v220", "v221", "v222", "v223", "v224", "v225", "v226", "v227", "v228", "v229", "v230", "v231", "v232", "v233", "v234", "v235", "v236", "v237", "v238", "v239", "v240", "v241", "v242", "v243", "v244", "v245", "v246", "v247", "v248", "v249", "v250", "v251", "v252", "v253", "v254", "v255", "s36", "s40", "s41", "s42", "s43", "s44", "s45", "s46", "s47", "scc", "memory", "vcc"
  );
  STAMP(ws, 0, 7);
  f32x4 a[5];
#pragma unroll
  for (int i = 0; i < 5; ++i) {
    const LAS char* p = sm + lo[i];
    a[i] = (*(const LAS f32x4*)p + *(const LAS f32x4*)(p + 20480)) + (*(const LAS f32x4*)(p + 40960) + *(const LAS f32x4*)(p + 61440));
  }
#pragma unroll
  for (int i = 0; i < 5; ++i) {
    const unsigned x0 = i < 4 ? __float_as_uint(res[2 * i]) : res4[0], x1 = i < 4 ? __float_as_uint(res[2 * i + 1]) : res4[1];
    st64(__builtin_amdgcn_make_buffer_rsrc(out, 0, S_ * D_ * 2, 0x00020000), go[i], (tok0 * D_ + col0) * 2,
         u32x2{pk_bf16(a[i][0] + bf_lo(x0), a[i][1] + bf_hi(x0)), pk_bf16(a[i][2] + bf_lo(x1), a[i][3] + bf_hi(x1))});
  }
}

__global__ __launch_bounds__(256, 2) void k_all(const u16* __restrict__ x, const u16* __restrict__ wqkv, const u16* __restrict__ wo_, const u16* __restrict__ w1,
                                                const u16* __restrict__ w3, const u16* __restrict__ w2, char* __restrict__ ws, u16* __restrict__ out) {
  extern __shared__ __attribute__((aligned(16))) char smem[];
  LAS char* sm = (LAS char*)smem;
  const int b = blockIdx.x, lane = threadIdx.x & 63, w = __builtin_amdgcn_readfirstlane(threadIdx.x >> 6);
  uintptr_t pc; asm volatile("s_getpc_b64 %0" : "=s"(pc));
  asm volatile("" :: "s"(x), "s"(wqkv), "s"(ws));
  STAMP(ws, 0, 0);
  if (__builtin_expect(b < 256, 1)) {
    __builtin_amdgcn_s_setprio(PRIO_A);
    const int L = (b & 7) * 30 + (b >> 3), rt = (L >= 120 ? 8 : 0) + (L & 7), ct = (L >= 120 ? L - 120 : L) >> 3;
    u64 tag;
    const int head = (b & 7) * 4 + ((b >> 3) & 3), qb = b >> 5;
    if (__builtin_expect(b < 240, 1)) {
      const QkvSrc src = qkv_start(x, wqkv, sm, rt * 128, ct * 128, ROT ? (rt + 3 * ct) % 10 : 0, w, lane);
      STAMP(ws, 2, 0);
      if (ct >= 10) tag = qkv_tile<true>(src, sm, ws, rt * 128, ct * 128, w, lane);
      else tag = qkv_tile<false>(src, sm, ws, rt * 128, ct * 128, w, lane);
    } else {
      const u64 t0 = __builtin_amdgcn_s_memrealtime();
      tag = uniform64(ld_flag(ws, WS_EP)) + 1;
      while (__builtin_amdgcn_s_memrealtime() - t0 < GATE1) __builtin_amdgcn_s_sleep(4);
      pretile(wo_, w2, ws, sm + w * 20480, b - 240, w, lane);
    }
    STAMP(ws, 0, 3);
    if (GPOS == 2 && w == 0 && lane == 0) st_flag(ws, WS_GF + b * 128, tag);
    if (b < 240) publish(ws, WS_QKF + (ct * 16 + rt) * QS, tag, w);
    STAMP(ws, 1, 2);
    if (GPOS == 0 && w == 0 && lane == 0) st_flag(ws, WS_GF + b * 128, tag);
    attn_unit(ws, sm, head, qb, w, lane, tag);
    STAMP(ws, 1, 1);
    publish(ws, WS_OF + (qb * 32 + head) * OS, tag, w);
    STAMP(ws, 0, 7);
  } else {
    __builtin_amdgcn_s_setprio(PRIO_LN);
    const u64 t0 = __builtin_amdgcn_s_memrealtime();
    const int i = b - 256, xc = i & 7, j = i >> 3;
    u64 tag;
    const int lg = LNMAP ? (xc >> 2) * 128 + (xc & 3) * 32 + j : i;
    ln_rows(x, ws, lg * 8, (LAS f32x2*)sm, w, tag);
    publish(ws, WS_LNF + lg * 8, tag, w);
    STAMP(ws, 0, 1);
    const int rt = (xc & 1) * 8 + (j & 7), ct = (xc >> 1) * 4 + (j >> 3);
    if (CODE_KB && j * 32 + w * 8 < CODE_KB) {
      asm volatile("s_mov_b32 m0, %2\n\ts_nop 0\n\tglobal_load_lds_dword %0, %1" :: "v"(lane * 128), "s"((pc & ~(uintptr_t)255) + (j * 32 + w * 8) * 1024), "s"(lds_addr(sm) + 4096) : "memory");
    }
    const int ort = (xc >> 1) * 8 + (j & 7), oct = (xc & 1) * 4 + (j >> 3);
    const char* wo_img = ws + WS_WT + (oct * 68 + 48 + w * 5) * 5120 + 2048;
    WoRegs wq;
    gateup_tile(w1, w3, ws, sm, rt, ct, ROTU == 1 ? ((j & 7) + 3 * (j >> 3)) % 10 : ROTU == 2 ? (j & 1) * 5 : ROTU == 3 ? (j & 3) * 2 : 0, wo_img, wq, w, tag, t0, WS_GF + i * 128);
    asm volatile("s_waitcnt vmcnt(0)" : WQ_OPS("+") :: "memory");
    publish(ws, WS_HF + (rt * 16 + ct) * HS, tag, w);
    STAMP(ws, 0, 4);
    out_tile(x, ws, out, sm, i == 0, ort, oct, wo_img, wq, w, tag);
    STAMP(ws, 1, 0);
  }
}

extern "C" void kernel_launch(void* const* d_in, const int* in_sizes, int n_in, void* d_out, int out_size, void* d_ws, size_t ws_size, hipStream_t stream) {
  (void)hipFuncSetAttribute((const void*)k_all, hipFuncAttributeMaxDynamicSharedMemorySize, 81920);
  k_all<<<512, 256, 81920, stream>>>((const u16*)d_in[0], (const u16*)d_in[1], (const u16*)d_in[2], (const u16*)d_in[4], (const u16*)d_in[5], (const u16*)d_in[6], (char*)d_ws, (u16*)d_out);
}
```
